```python
import torch

def _softmax(x, axis=-1):
    return torch.softmax(x, dim=axis)
def _gelu(x):
    return torch.nn.functional.gelu(x, approximate="tanh")
def _silu(x):
    return torch.nn.functional.silu(x)
def _sigmoid(x):
    return torch.sigmoid(x)
def _layernorm(x, axis=-1, eps=1e-5):
    m = x.mean(dim=axis, keepdim=True)
    v = x.var(dim=axis, keepdim=True, unbiased=False)
    return (x - m) / torch.sqrt(v + eps)
def _scatter_add(tgt, idx, src, axis=0):
    return tgt.clone().index_add_(axis, idx, src)
def _segment_sum(src, seg, n):
    out = torch.zeros((n,) + src.shape[1:], dtype=src.dtype)
    return out.index_add_(0, seg, src)
def _pad(x, pw):
    flat = tuple(v for pair in reversed(pw) for v in pair)
    return torch.nn.functional.pad(x, flat)

kernel_name = 'recurrent_scan__compressed_kv_cla__dense_activation_mish__prompt'

def setup_inputs():

    _w = (lambda a: (lambda t: t.to(torch.bfloat16) if t.is_floating_point() else t)(torch.as_tensor(a)))
    def _A_setup():
        B, H, Sq, Sk, D, M = 1, 16, 2048, 2048, 256, 448
        Dc = 4 * D
        return {
            "x": _w((lambda *s: torch.randn(*s, dtype=torch.bfloat16) * 0.05)(B, Sq, M)),
            "c_kv": _w((lambda *s: torch.randn(*s, dtype=torch.bfloat16) * 0.8)(B, Sk, Dc)),
            "w_q": _w((lambda *s: torch.randn(*s, dtype=torch.bfloat16) * 1.6)(M, H, D) * (M ** -0.5)),
            "w_uk": _w((lambda *s: torch.randn(*s, dtype=torch.bfloat16) * 1.6)(Dc, H, D) * (Dc ** -0.5)),
            "w_uv": _w((lambda *s: torch.randn(*s, dtype=torch.bfloat16) * 0.8)(Dc, H, D) * (Dc ** -0.5)),
            "w_o": _w((lambda *s: torch.randn(*s, dtype=torch.bfloat16) * 0.8)(H, D, M) * ((H * D) ** -0.5)),
        }
    _A_in = _A_setup()

    def _B_setup():
        B, S, D, F = 1, 2048, 448, 1792
        return {
            "x": _w((lambda *s: torch.randn(*s, dtype=torch.bfloat16) * 0.05)(B, S, D)),
            "w1": _w((lambda *s: torch.randn(*s, dtype=torch.bfloat16) * 0.8)(D, F) * (D ** -0.5)),
            "b1": _w((lambda *s: torch.randn(*s, dtype=torch.bfloat16) * 0.8)(F)),
            "w2": _w((lambda *s: torch.randn(*s, dtype=torch.bfloat16) * 0.2)(F, D) * (F ** -0.5)),
        }
    _B_in = _B_setup()

    return {
        "A_x": _A_in["x"],
        "A_c_kv": _A_in["c_kv"],
        "A_w_q": _A_in["w_q"],
        "A_w_uk": _A_in["w_uk"],
        "A_w_uv": _A_in["w_uv"],
        "A_w_o": _A_in["w_o"],
        "B_x": _B_in["x"],
        "B_w1": _B_in["w1"],
        "B_b1": _B_in["b1"],
        "B_w2": _B_in["w2"],
    }

def reference(A_x, A_c_kv, A_w_q, A_w_uk, A_w_uv, A_w_o, B_x, B_w1, B_b1, B_w2):
    def _A_ref(x, c_kv, w_q, w_uk, w_uv, w_o):
        B, Sq, M = x.shape[0], x.shape[1], x.shape[2]
        Sk = c_kv.shape[1]
        H, D = w_q.shape[1], w_q.shape[2]
        q = torch.einsum("bqm,mhd->bhqd", x, w_q)
        k = torch.einsum("bkc,chd->bhkd", c_kv, w_uk)
        v = torch.einsum("bkc,chd->bhkd", c_kv, w_uv)
        scores = torch.einsum("bhqd,bhkd->bhqk", q, k) * (D ** -0.5)
        off = Sk - Sq
        qi = torch.arange(Sq)[:, None]
        kj = torch.arange(Sk)[None, :]
        cmask = kj <= (qi + off)
        scores = torch.where(cmask[None, None], scores, -1e9)
        p = _softmax(scores, axis=-1)
        ctx = torch.einsum("bhqk,bhkd->bhqd", p, v)
        return torch.einsum("bhqd,hdm->bqm", ctx, w_o)

    def _B_ref(x, w1, b1, w2):
        h = torch.einsum("bsd,df->bsf", x, w1) + b1
        sp = torch.log1p(torch.exp(-torch.abs(h))) + torch.where(h > 0, h, torch.zeros_like(h))
        act = h * torch.tanh(sp)
        return torch.einsum("bsf,fd->bsd", act, w2)

    x = A_x
    xn = _layernorm(x, axis=-1)
    s = _A_ref(xn, A_c_kv, A_w_q, A_w_uk, A_w_uv, A_w_o)
    g = _silu(_B_ref(xn, B_w1, B_b1, B_w2))
    return x + s * g
```

```cpp
#include <hip/hip_runtime.h>
#include <hip/hip_bf16.h>
#include <stdint.h>

typedef unsigned short u16;
typedef short bf16x8 __attribute__((ext_vector_type(8)));
typedef short s16x4 __attribute__((ext_vector_type(4)));
typedef float f32x4 __attribute__((ext_vector_type(4)));
typedef float f32x16 __attribute__((ext_vector_type(16)));
typedef unsigned u32x2 __attribute__((ext_vector_type(2)));
typedef unsigned u32x4 __attribute__((ext_vector_type(4)));
typedef __attribute__((address_space(3))) s16x4 lds_s16x4;

constexpr int S_ = 2048, M_ = 448, H_ = 16, D_ = 256, DC_ = 1024, F_ = 1792;
constexpr int HD_ = H_ * D_;

constexpr size_t WS_CNT  = 0;
constexpr size_t WS_XN   = 4096;
constexpr size_t WS_Q    = WS_XN + (size_t)S_ * M_ * 2;
constexpr size_t WS_K    = WS_Q + (size_t)S_ * HD_ * 2;
constexpr size_t WS_VT   = WS_K + (size_t)S_ * HD_ * 2;
constexpr size_t WS_ACT  = WS_VT + (size_t)S_ * HD_ * 2;
constexpr size_t WS_CTX  = WS_ACT + (size_t)S_ * F_ * 2;
constexpr size_t WS_PART = WS_CTX + (size_t)S_ * HD_ * 2;
constexpr size_t WS_PML  = WS_PART + (size_t)128 * 131072;
constexpr size_t WS_END  = WS_PML + (size_t)128 * 256 * 8;

__device__ __forceinline__ float bf2f(u16 v) { return __uint_as_float(((unsigned)v) << 16); }
__device__ __forceinline__ u16 f2bf(float f) {
  unsigned u = __float_as_uint(f);
  u += 0x7FFFu + ((u >> 16) & 1u);
  return (u16)(u >> 16);
}
__device__ __forceinline__ float rbf(float f) { return bf2f(f2bf(f)); }
typedef float f32x2 __attribute__((ext_vector_type(2)));
typedef __bf16 bf16x2_t __attribute__((ext_vector_type(2)));
__device__ __forceinline__ unsigned pk2(float lo, float hi) {
  f32x2 v = {lo, hi};
  bf16x2_t b = __builtin_convertvector(v, bf16x2_t);
  return *reinterpret_cast<unsigned*>(&b);
}
__device__ __forceinline__ int crow(int r, int hi) { return (r & 3) + 8 * (r >> 2) + 4 * hi; }

#define GLDS16(gptr, ldsptr) \
  __builtin_amdgcn_global_load_lds((const __attribute__((address_space(1))) unsigned*)(gptr), \
                                   (__attribute__((address_space(3))) unsigned*)(ldsptr), 16, 0, 0)

__global__ __launch_bounds__(256) void ln_kernel(const u16* __restrict__ x, u16* __restrict__ xn, unsigned* __restrict__ cnt) {
  const int wave = threadIdx.x >> 6, lane = threadIdx.x & 63;
  const int row = blockIdx.x * 4 + wave;
  if (blockIdx.x == 0) cnt[threadIdx.x] = 0;
  float v[8];
  const bool act = lane < 56;
  if (act) {
    u32x4 w = *reinterpret_cast<const u32x4*>(x + (size_t)row * M_ + lane * 8);
#pragma unroll
    for (int i = 0; i < 4; ++i) { v[2 * i] = __uint_as_float(w[i] << 16); v[2 * i + 1] = __uint_as_float(w[i] & 0xffff0000u); }
  } else {
#pragma unroll
    for (int i = 0; i < 8; ++i) v[i] = 0.f;
  }
  float s = 0.f;
#pragma unroll
  for (int i = 0; i < 8; ++i) s += v[i];
#pragma unroll
  for (int o = 32; o >= 1; o >>= 1) s += __shfl_xor(s, o);
  const float mean = s * (1.f / M_);
  float q = 0.f;
  if (act) {
#pragma unroll
    for (int i = 0; i < 8; ++i) { float d = v[i] - mean; q += d * d; }
  }
#pragma unroll
  for (int o = 32; o >= 1; o >>= 1) q += __shfl_xor(q, o);
  const float var = q * (1.f / M_);
  const float mb = rbf(mean), vb = rbf(var);
  const float den = rbf(sqrtf(rbf(vb + 1e-5f)));
  if (act) {
    u32x4 o;
#pragma unroll
    for (int i = 0; i < 4; ++i) {
      float a = rbf(rbf(v[2 * i] - mb) / den), b = rbf(rbf(v[2 * i + 1] - mb) / den);
      o[i] = (unsigned)f2bf(a) | ((unsigned)f2bf(b) << 16);
    }
    *reinterpret_cast<u32x4*>(xn + (size_t)row * M_ + lane * 8) = o;
  }
}

__device__ __forceinline__ unsigned offB(unsigned row, unsigned ch) { return 256u * row + 16u * (ch ^ (((row & 3) << 2) | ((row >> 2) & 3))); }

template <int MODE>
__device__ __forceinline__ void gemm128(const u16* __restrict__ A, int lda, const u16* __restrict__ B, int ldb, int nt,
                                        int m0, int n0, char* smem, u16* __restrict__ out, const u16* __restrict__ bias) {
  const int tid = threadIdx.x, wid = tid >> 6, lane = tid & 63;
  const int wm = wid >> 1, wn = wid & 1;
  const int fr = lane & 15, fq = lane >> 4;
  char* sA = smem;
  char* sB = smem + 32768;
  f32x4 acc[4][4];
#pragma unroll
  for (int i = 0; i < 4; ++i)
#pragma unroll
    for (int j = 0; j < 4; ++j) acc[i][j] = f32x4{0.f, 0.f, 0.f, 0.f};

  auto stage = [&](int buf, int kt) {
#pragma unroll
    for (int i = 0; i < 4; ++i) {
      const int p = i * 256 + tid;
      { const int row = p >> 3, chs = (p & 7) ^ ((row >> 1) & 7);
        GLDS16(A + (size_t)(m0 + row) * lda + kt * 64 + chs * 8, sA + buf * 16384 + p * 16); }
      { const int row = p >> 4, chs = (p & 15) ^ (((row & 3) << 2) | ((row >> 2) & 3));
        GLDS16(B + (size_t)(kt * 64 + row) * ldb + n0 + chs * 8, sB + buf * 16384 + p * 16); }
    }
  };
  stage(0, 0);
  asm volatile("s_waitcnt vmcnt(0)" ::: "memory");
  __syncthreads();
  for (int kt = 0; kt < nt; ++kt) {
    const int buf = kt & 1;
    if (kt + 1 < nt) stage(buf ^ 1, kt + 1);
    const char* a = sA + buf * 16384;
    const char* b = sB + buf * 16384;
#pragma unroll
    for (int ks = 0; ks < 2; ++ks) {
      bf16x8 af[4], bfr[4];
#pragma unroll
      for (int mi = 0; mi < 4; ++mi) {
        const int row = wm * 64 + mi * 16 + fr, ch = ks * 4 + fq;
        af[mi] = *reinterpret_cast<const bf16x8*>(a + row * 128 + ((ch ^ ((row >> 1) & 7)) << 4));
      }
#pragma unroll
      for (int ni = 0; ni < 4; ++ni) {
        const int c = wn * 4 + ni, q = fr >> 2, pp = lane & 3;
        const unsigned a0 = offB(32 * ks + 8 * fq + q, 2 * c + (pp >> 1)) + 8 * (pp & 1);
        const unsigned a1 = offB(32 * ks + 8 * fq + 4 + q, 2 * c + (pp >> 1)) + 8 * (pp & 1);
        s16x4 lo = __builtin_amdgcn_ds_read_tr16_b64_v4i16((lds_s16x4*)(b + a0));
        s16x4 hi = __builtin_amdgcn_ds_read_tr16_b64_v4i16((lds_s16x4*)(b + a1));
        bfr[ni] = bf16x8{lo[0], lo[1], lo[2], lo[3], hi[0], hi[1], hi[2], hi[3]};
      }
#pragma unroll
      for (int mi = 0; mi < 4; ++mi)
#pragma unroll
        for (int ni = 0; ni < 4; ++ni) {
          if (MODE == 1) acc[mi][ni] = __builtin_amdgcn_mfma_f32_16x16x32_bf16(af[mi], bfr[ni], acc[mi][ni], 0, 0, 0);
          else           acc[mi][ni] = __builtin_amdgcn_mfma_f32_16x16x32_bf16(bfr[ni], af[mi], acc[mi][ni], 0, 0, 0);
        }
    }
    asm volatile("s_waitcnt vmcnt(0)" ::: "memory");
    __syncthreads();
  }
#pragma unroll
  for (int mi = 0; mi < 4; ++mi)
#pragma unroll
    for (int ni = 0; ni < 4; ++ni) {
      const f32x4 v = acc[mi][ni];
      if (MODE == 1) {
        const int n = n0 + wn * 64 + ni * 16 + fr;
        const int m = m0 + wm * 64 + mi * 16 + fq * 4;
        const int h = n >> 8, d = n & 255, tile = m >> 6, kk = m & 63;
        const int ks = kk >> 4, qq = (kk >> 2) & 3;
        const int half = qq & 1, sub = qq >> 1;
        const int ch = ks * 2 + half;
        const size_t off = (((size_t)(h * 32 + tile) * 256 + d) * 64) + ((ch ^ ((d >> 1) & 7)) << 3) + sub * 4;
        u32x2 w = {pk2(v[0], v[1]), pk2(v[2], v[3])};
        *reinterpret_cast<u32x2*>(out + off) = w;
      } else {
        const int m = m0 + wm * 64 + mi * 16 + fr;
        const int n = n0 + wn * 64 + ni * 16 + fq * 4;
        if (MODE == 0) {
          const int h = n >> 8, d = n & 255, ch = d >> 3;
          const size_t off = ((size_t)h * S_ + m) * 256 + ((ch ^ (m & 15)) << 3) + (d & 7);
          u32x2 w = {pk2(v[0], v[1]), pk2(v[2], v[3])};
          *reinterpret_cast<u32x2*>(out + off) = w;
        } else if (MODE == 2) {
          u32x2 w = {pk2(v[0], v[1]), pk2(v[2], v[3])};
          *reinterpret_cast<u32x2*>(out + (size_t)m * HD_ + n) = w;
        } else {
          float r[4];
#pragma unroll
          for (int j = 0; j < 4; ++j) {
            const float hh = rbf(rbf(v[j]) + bf2f(bias[n + j]));
            const float sp = log1pf(__expf(-fabsf(hh))) + fmaxf(hh, 0.f);
            r[j] = hh * tanhf(sp);
          }
          u32x2 w = {pk2(r[0], r[1]), pk2(r[2], r[3])};
          *reinterpret_cast<u32x2*>(out + (size_t)m * F_ + n) = w;
        }
      }
    }
}

__global__ __launch_bounds__(256, 2) void proj_kernel(const u16* __restrict__ xn, const u16* __restrict__ ckv,
                                                      const u16* __restrict__ wq, const u16* __restrict__ wuk,
                                                      const u16* __restrict__ wuv, const u16* __restrict__ w1,
                                                      const u16* __restrict__ b1, char* __restrict__ ws) {
  extern __shared__ __attribute__((aligned(16))) char smem[];
  int b = blockIdx.x;
  if (b < 512) {
    gemm128<0>(ckv, DC_, wuk, HD_, DC_ / 64, (b & 15) * 128, (b >> 4) * 128, smem, (u16*)(ws + WS_K), nullptr);
  } else if (b < 1024) {
    b -= 512;
    gemm128<1>(ckv, DC_, wuv, HD_, DC_ / 64, (b & 15) * 128, (b >> 4) * 128, smem, (u16*)(ws + WS_VT), nullptr);
  } else if (b < 1536) {
    b -= 1024;
    gemm128<2>(xn, M_, wq, HD_, M_ / 64, (b & 15) * 128, (b >> 4) * 128, smem, (u16*)(ws + WS_Q), nullptr);
  } else {
    b -= 1536;
    gemm128<3>(xn, M_, w1, F_, M_ / 64, (b & 15) * 128, (b >> 4) * 128, smem, (u16*)(ws + WS_ACT), b1);
  }
}

constexpr float SC2 = 0.0625f * 1.4426950408889634f;

struct AttnState { f32x16 o[8]; float m, l; };

__device__ __forceinline__ void attn_stage_k(const u16* __restrict__ Kh, int tile, char* kbuf, int tid) {
  const u16* src = Kh + (size_t)tile * 64 * 256;
#pragma unroll
  for (int i = 0; i < 8; ++i) { const int p = i * 256 + tid; GLDS16(src + p * 8, kbuf + p * 16); }
}
__device__ __forceinline__ void attn_qk(f32x16& p0, f32x16& p1, const char* kb, const bf16x8* qr, int r32, int hi) {
  p0 = f32x16{}; p1 = f32x16{};
#pragma unroll
  for (int st = 0; st < 16; ++st) {
    const int ch = st * 2 + hi;
    const bf16x8 k0 = *reinterpret_cast<const bf16x8*>(kb + r32 * 512 + ((ch ^ (r32 & 15)) << 4));
    const bf16x8 k1 = *reinterpret_cast<const bf16x8*>(kb + (32 + r32) * 512 + ((ch ^ (r32 & 15)) << 4));
    p0 = __builtin_amdgcn_mfma_f32_32x32x16_bf16(k0, qr[st], p0, 0, 0, 0);
    p1 = __builtin_amdgcn_mfma_f32_32x32x16_bf16(k1, qr[st], p1, 0, 0, 0);
  }
}

__device__ __forceinline__ void attn_segment(AttnState& st, const u16* __restrict__ Qb, const u16* __restrict__ Kh,
                                             const u16* __restrict__ Vh, int h, int qb, int t0, int t1, char* smem) {
  const int tid = threadIdx.x, wid = tid >> 6, lane = tid & 63, r32 = lane & 31, hi = lane >> 5;
  char* sK = smem;
  char* sV = smem + 65536;
  const int qw0 = qb * 128 + wid * 32;
  bf16x8 qr[16];
  {
    const u16* qp = Qb + (size_t)(qw0 + r32) * HD_ + h * 256 + hi * 8;
#pragma unroll
    for (int s = 0; s < 16; ++s) qr[s] = *reinterpret_cast<const bf16x8*>(qp + s * 16);
  }
#pragma unroll
  for (int d = 0; d < 8; ++d) st.o[d] = f32x16{};
  st.m = -1e30f; st.l = 0.f;
  __syncthreads();
  attn_stage_k(Kh, t0, sK + (t0 & 1) * 32768, tid);
  attn_stage_k(Vh, t0, sV + (t0 & 1) * 32768, tid);
  if (t0 + 1 < t1) attn_stage_k(Kh, t0 + 1, sK + ((t0 + 1) & 1) * 32768, tid);
  asm volatile("s_waitcnt vmcnt(0)" ::: "memory");
  __syncthreads();
  f32x16 p0, p1;
  attn_qk(p0, p1, sK + (t0 & 1) * 32768, qr, r32, hi);
  __syncthreads();
  for (int j = t0; j < t1; ++j) {
    if (j + 2 < t1) attn_stage_k(Kh, j + 2, sK + (j & 1) * 32768, tid);
    if (j + 1 < t1) attn_stage_k(Vh, j + 1, sV + ((j + 1) & 1) * 32768, tid);
    const int k0 = j * 64;
    if (k0 + 63 > qw0) {
      const int q = qw0 + r32;
#pragma unroll
      for (int r = 0; r < 16; ++r) {
        if (k0 + crow(r, hi) > q) p0[r] = -INFINITY;
        if (k0 + 32 + crow(r, hi) > q) p1[r] = -INFINITY;
      }
    }
    float pmax = p0[0];
#pragma unroll
    for (int r = 1; r < 16; ++r) pmax = fmaxf(pmax, p0[r]);
#pragma unroll
    for (int r = 0; r < 16; ++r) pmax = fmaxf(pmax, p1[r]);
    pmax = fmaxf(pmax, __shfl_xor(pmax, 32));
    const float mn = fmaxf(st.m, pmax);
    const float alpha = __builtin_amdgcn_exp2f((st.m - mn) * SC2);
    st.m = mn;
    const float mc = -mn * SC2;
    float ps = 0.f;
#pragma unroll
    for (int r = 0; r < 16; ++r) { p0[r] = __builtin_amdgcn_exp2f(fmaf(p0[r], SC2, mc)); ps += p0[r]; }
#pragma unroll
    for (int r = 0; r < 16; ++r) { p1[r] = __builtin_amdgcn_exp2f(fmaf(p1[r], SC2, mc)); ps += p1[r]; }
    ps += __shfl_xor(ps, 32);
    st.l = st.l * alpha + ps;
    bf16x8 pf[4];
    {
      u32x4 w0 = {pk2(p0[0], p0[1]), pk2(p0[2], p0[3]), pk2(p0[4], p0[5]), pk2(p0[6], p0[7])};
      u32x4 w1 = {pk2(p0[8], p0[9]), pk2(p0[10], p0[11]), pk2(p0[12], p0[13]), pk2(p0[14], p0[15])};
      u32x4 w2 = {pk2(p1[0], p1[1]), pk2(p1[2], p1[3]), pk2(p1[4], p1[5]), pk2(p1[6], p1[7])};
      u32x4 w3 = {pk2(p1[8], p1[9]), pk2(p1[10], p1[11]), pk2(p1[12], p1[13]), pk2(p1[14], p1[15])};
      pf[0] = *reinterpret_cast<bf16x8*>(&w0); pf[1] = *reinterpret_cast<bf16x8*>(&w1);
      pf[2] = *reinterpret_cast<bf16x8*>(&w2); pf[3] = *reinterpret_cast<bf16x8*>(&w3);
    }
    if (__any(alpha < 1.f)) {
#pragma unroll
      for (int d = 0; d < 8; ++d)
#pragma unroll
        for (int r = 0; r < 16; ++r) st.o[d][r] *= alpha;
    }
    if (j + 1 < t1) attn_qk(p0, p1, sK + ((j + 1) & 1) * 32768, qr, r32, hi);
    const char* vb = sV + (j & 1) * 32768;
#pragma unroll
    for (int d = 0; d < 8; ++d) {
      const int row = d * 32 + r32;
#pragma unroll
      for (int ks = 0; ks < 4; ++ks) {
        const bf16x8 vf = *reinterpret_cast<const bf16x8*>(vb + row * 128 + (((ks * 2 + hi) ^ ((row >> 1) & 7)) << 4));
        st.o[d] = __builtin_amdgcn_mfma_f32_32x32x16_bf16(vf, pf[ks], st.o[d], 0, 0, 0);
      }
    }
    asm volatile("s_waitcnt vmcnt(0)" ::: "memory");
    __syncthreads();
  }
}

__device__ __forceinline__ void attn_write_ctx(const AttnState& st, u16* __restrict__ ctx, int h, int qb) {
  const int tid = threadIdx.x, wid = tid >> 6, lane = tid & 63, r32 = lane & 31, hi = lane >> 5;
  const float inv = 1.f / st.l;
  u16* dst = ctx + (size_t)(qb * 128 + wid * 32 + r32) * HD_ + h * 256 + 4 * hi;
#pragma unroll
  for (int d = 0; d < 8; ++d)
#pragma unroll
    for (int g = 0; g < 4; ++g) {
      u32x2 w = {pk2(st.o[d][4 * g] * inv, st.o[d][4 * g + 1] * inv), pk2(st.o[d][4 * g + 2] * inv, st.o[d][4 * g + 3] * inv)};
      *reinterpret_cast<u32x2*>(dst + d * 32 + 8 * g) = w;
    }
}

__global__ __launch_bounds__(256, 1) void attn_kernel(char* __restrict__ ws) {
  extern __shared__ __attribute__((aligned(16))) char smem[];
  const u16* Qb = (const u16*)(ws + WS_Q);
  const u16* Kb = (const u16*)(ws + WS_K);
  const u16* Vt = (const u16*)(ws + WS_VT);
  u16* ctx = (u16*)(ws + WS_CTX);
  unsigned* cnt = (unsigned*)(ws + WS_CNT);
  const int xg = blockIdx.x & 7, r = blockIdx.x >> 3;
  const int h = xg * 2 + (r & 1), rr = r >> 1;
  const int i = rr >> 1, side = rr & 1;
  const u16* Kh = Kb + (size_t)h * S_ * 256;
  const u16* Vh = Vt + (size_t)h * S_ * 256;
  const int tid = threadIdx.x, wid = tid >> 6, lane = tid & 63;
  AttnState st;
  if (side == 1) {
    attn_segment(st, Qb, Kh, Vh, h, i, 0, 2 * (i + 1), smem);
    attn_write_ctx(st, ctx, h, i);
  }
  const int qb = 15 - i;
  if (side == 0) attn_segment(st, Qb, Kh, Vh, h, qb, 0, 17, smem);
  else           attn_segment(st, Qb, Kh, Vh, h, qb, 17, 2 * (16 - i), smem);
  const int pair = h * 8 + i;
  volatile unsigned* s_ticket = reinterpret_cast<volatile unsigned*>(smem);
  if (tid == 0) *s_ticket = atomicAdd(&cnt[pair], 1u);
  __syncthreads();
  const unsigned ticket = *s_ticket;
  f32x4* part = reinterpret_cast<f32x4*>(ws + WS_PART + (size_t)pair * 131072) + wid * 2048 + lane;
  float2* pml = reinterpret_cast<float2*>(ws + WS_PML) + pair * 256 + tid;
  if (ticket == 0) {
#pragma unroll
    for (int d = 0; d < 8; ++d)
#pragma unroll
      for (int g = 0; g < 4; ++g)
        part[(d * 4 + g) * 64] = f32x4{st.o[d][4 * g], st.o[d][4 * g + 1], st.o[d][4 * g + 2], st.o[d][4 * g + 3]};
    *pml = float2{st.m, st.l};
    asm volatile("s_waitcnt vmcnt(0)" ::: "memory");
    __syncthreads();
    if (tid == 0) {
      __builtin_amdgcn_fence(__ATOMIC_RELEASE, "agent");
      asm volatile("s_waitcnt vmcnt(0)" ::: "memory");
      __hip_atomic_store(&cnt[128 + pair], 1u, __ATOMIC_RELAXED, __HIP_MEMORY_SCOPE_AGENT);
    }
  } else {
    if (tid == 0) {
      while (__hip_atomic_load(&cnt[128 + pair], __ATOMIC_RELAXED, __HIP_MEMORY_SCOPE_AGENT) == 0u) __builtin_amdgcn_s_sleep(2);
      __builtin_amdgcn_fence(__ATOMIC_ACQUIRE, "agent");
      asm volatile("s_waitcnt vmcnt(0)" ::: "memory");
    }
    __syncthreads();
    const float2 ml = *pml;
    const float mn = fmaxf(st.m, ml.x);
    const float a1 = __builtin_amdgcn_exp2f((st.m - mn) * SC2), a2 = __builtin_amdgcn_exp2f((ml.x - mn) * SC2);
    st.l = st.l * a1 + ml.y * a2;
#pragma unroll
    for (int d = 0; d < 8; ++d)
#pragma unroll
      for (int g = 0; g < 4; ++g) {
        const f32x4 v = part[(d * 4 + g) * 64];
#pragma unroll
        for (int j = 0; j < 4; ++j) st.o[d][4 * g + j] = st.o[d][4 * g + j] * a1 + v[j] * a2;
      }
    attn_write_ctx(st, ctx, h, qb);
  }
}

__device__ __forceinline__ unsigned offB64(unsigned row, unsigned ch) {
  const unsigned f = ((row >> 1) & 1) | (((row >> 3) & 1) << 1);
  return 128u * row + 16u * (ch ^ (f << 1));
}
__device__ __forceinline__ void out_loop(f32x4 (&acc)[4][2], const u16* __restrict__ A, int lda, const u16* __restrict__ B,
                                         int nt, int m0, int n0, char* smem) {
  const int tid = threadIdx.x, wid = tid >> 6, lane = tid & 63;
  const int wm = wid >> 1, wn = wid & 1, fr = lane & 15, fq = lane >> 4;
  char* sA = smem;
  char* sB = smem + 32768;
  auto stage = [&](int buf, int kt) {
#pragma unroll
    for (int i = 0; i < 4; ++i) {
      const int p = i * 256 + tid;
      const int row = p >> 3, chs = (p & 7) ^ ((row >> 1) & 7);
      GLDS16(A + (size_t)(m0 + row) * lda + kt * 64 + chs * 8, sA + buf * 16384 + p * 16);
    }
#pragma unroll
    for (int i = 0; i < 2; ++i) {
      const int p = i * 256 + tid;
      const int row = p >> 3; const unsigned f = ((row >> 1) & 1) | (((row >> 3) & 1) << 1);
      const int chs = (p & 7) ^ (f << 1);
      GLDS16(B + (size_t)(kt * 64 + row) * M_ + n0 + chs * 8, sB + buf * 8192 + p * 16);
    }
  };
  __syncthreads();
  stage(0, 0);
  asm volatile("s_waitcnt vmcnt(0)" ::: "memory");
  __syncthreads();
  for (int kt = 0; kt < nt; ++kt) {
    const int buf = kt & 1;
    if (kt + 1 < nt) stage(buf ^ 1, kt + 1);
    const char* a = sA + buf * 16384;
    const char* b = sB + buf * 8192;
#pragma unroll
    for (int ks = 0; ks < 2; ++ks) {
      bf16x8 af[4], bfr[2];
#pragma unroll
      for (int mi = 0; mi < 4; ++mi) {
        const int row = wm * 64 + mi * 16 + fr, ch = ks * 4 + fq;
        af[mi] = *reinterpret_cast<const bf16x8*>(a + row * 128 + ((ch ^ ((row >> 1) & 7)) << 4));
      }
#pragma unroll
      for (int ni = 0; ni < 2; ++ni) {
        const int c = wn * 2 + ni, q = fr >> 2, pp = lane & 3;
        const unsigned a0 = offB64(32 * ks + 8 * fq + q, 2 * c + (pp >> 1)) + 8 * (pp & 1);
        const unsigned a1 = offB64(32 * ks + 8 * fq + 4 + q, 2 * c + (pp >> 1)) + 8 * (pp & 1);
        s16x4 lo = __builtin_amdgcn_ds_read_tr16_b64_v4i16((lds_s16x4*)(b + a0));
        s16x4 hi = __builtin_amdgcn_ds_read_tr16_b64_v4i16((lds_s16x4*)(b + a1));
        bfr[ni] = bf16x8{lo[0], lo[1], lo[2], lo[3], hi[0], hi[1], hi[2], hi[3]};
      }
#pragma unroll
      for (int mi = 0; mi < 4; ++mi)
#pragma unroll
        for (int ni = 0; ni < 2; ++ni)
          acc[mi][ni] = __builtin_amdgcn_mfma_f32_16x16x32_bf16(bfr[ni], af[mi], acc[mi][ni], 0, 0, 0);
    }
    asm volatile("s_waitcnt vmcnt(0)" ::: "memory");
    __syncthreads();
  }
}

__global__ __launch_bounds__(256, 2) void out_kernel(const u16* __restrict__ x, const u16* __restrict__ wo, const u16* __restrict__ w2,
                                                     const char* __restrict__ ws, u16* __restrict__ out) {
  extern __shared__ __attribute__((aligned(16))) char smem[];
  const int m0 = (blockIdx.x & 15) * 128, n0 = (blockIdx.x >> 4) * 64;
  f32x4 accs[4][2], accg[4][2];
#pragma unroll
  for (int i = 0; i < 4; ++i)
#pragma unroll
    for (int j = 0; j < 2; ++j) { accs[i][j] = f32x4{0.f, 0.f, 0.f, 0.f}; accg[i][j] = f32x4{0.f, 0.f, 0.f, 0.f}; }
  out_loop(accs, (const u16*)(ws + WS_CTX), HD_, wo, HD_ / 64, m0, n0, smem);
  out_loop(accg, (const u16*)(ws + WS_ACT), F_, w2, F_ / 64, m0, n0, smem);
  const int tid = threadIdx.x, wid = tid >> 6, lane = tid & 63;
  const int wm = wid >> 1, wn = wid & 1, fr = lane & 15, fq = lane >> 4;
#pragma unroll
  for (int mi = 0; mi < 4; ++mi)
#pragma unroll
    for (int ni = 0; ni < 2; ++ni) {
      const int m = m0 + wm * 64 + mi * 16 + fr;
      const int n = n0 + wn * 32 + ni * 16 + fq * 4;
      const u32x2 xw = *reinterpret_cast<const u32x2*>(x + (size_t)m * M_ + n);
      float r[4];
#pragma unroll
      for (int j = 0; j < 4; ++j) {
        const float xv = (j & 1) ? __uint_as_float(xw[j >> 1] & 0xffff0000u) : __uint_as_float(xw[j >> 1] << 16);
        const float sb = rbf(accs[mi][ni][j]), gb = rbf(accg[mi][ni][j]);
        const float sg = rbf(gb / (1.f + __expf(-gb)));
        r[j] = xv + rbf(sb * sg);
      }
      u32x2 w = {pk2(r[0], r[1]), pk2(r[2], r[3])};
      *reinterpret_cast<u32x2*>(out + (size_t)m * M_ + n) = w;
    }
}

extern "C" void kernel_launch(void* const* d_in, const int* in_sizes, int n_in,
                              void* d_out, int out_size, void* d_ws, size_t ws_size,
                              hipStream_t stream) {
  const u16* x   = (const u16*)d_in[0];
  const u16* ckv = (const u16*)d_in[1];
  const u16* wq  = (const u16*)d_in[2];
  const u16* wuk = (const u16*)d_in[3];
  const u16* wuv = (const u16*)d_in[4];
  const u16* wo  = (const u16*)d_in[5];
  const u16* w1  = (const u16*)d_in[7];
  const u16* b1  = (const u16*)d_in[8];
  const u16* w2  = (const u16*)d_in[9];
  char* ws = (char*)d_ws;
  (void)hipFuncSetAttribute((const void*)attn_kernel, hipFuncAttributeMaxDynamicSharedMemorySize, 131072);
  (void)hipFuncSetAttribute((const void*)proj_kernel, hipFuncAttributeMaxDynamicSharedMemorySize, 65536);
  (void)hipFuncSetAttribute((const void*)out_kernel, hipFuncAttributeMaxDynamicSharedMemorySize, 49152);
  ln_kernel<<<S_ / 4, 256, 0, stream>>>(x, (u16*)(ws + WS_XN), (unsigned*)(ws + WS_CNT));
  proj_kernel<<<1536 + 16 * 14, 256, 65536, stream>>>((const u16*)(ws + WS_XN), ckv, wq, wuk, wuv, w1, b1, ws);
  attn_kernel<<<256, 256, 131072, stream>>>(ws);
  out_kernel<<<16 * 7, 256, 49152, stream>>>(x, wo, w2, ws, (u16*)d_out);
}
```

```cpp
#include <hip/hip_runtime.h>
#include <hip/hip_bf16.h>
#include <stdint.h>
#include <utility>

typedef unsigned short u16;
typedef short bf16x8 __attribute__((ext_vector_type(8)));
typedef short s16x4 __attribute__((ext_vector_type(4)));
typedef float f32x4 __attribute__((ext_vector_type(4)));
typedef float f32x16 __attribute__((ext_vector_type(16)));
typedef unsigned u32x2 __attribute__((ext_vector_type(2)));
typedef unsigned u32x4 __attribute__((ext_vector_type(4)));
typedef __attribute__((address_space(3))) s16x4 lds_s16x4;

constexpr int S_ = 2048, M_ = 448, H_ = 16, D_ = 256, DC_ = 1024, F_ = 1792;
constexpr int HD_ = H_ * D_;

constexpr size_t WS_CNT  = 0;
constexpr size_t WS_XN   = 4096;
constexpr size_t WS_Q    = WS_XN + (size_t)S_ * 512 * 2;
constexpr size_t WS_K    = WS_Q + (size_t)S_ * HD_ * 2;
constexpr size_t WS_VT   = WS_K + (size_t)S_ * HD_ * 2;
constexpr size_t WS_ACT  = WS_VT + (size_t)S_ * HD_ * 2;
constexpr size_t WS_CTX  = WS_ACT + (size_t)S_ * F_ * 2;
constexpr size_t WS_PART = WS_CTX + (size_t)S_ * HD_ * 2;
constexpr size_t WS_PML  = WS_PART + (size_t)128 * 131072;
constexpr size_t WS_END  = WS_PML + (size_t)128 * 256 * 8;
constexpr size_t WS_OPART = WS_END;

__device__ __forceinline__ float bf2f(u16 v) { return __uint_as_float(((unsigned)v) << 16); }
__device__ __forceinline__ u16 f2bf(float f) {
  unsigned u = __float_as_uint(f);
  u += 0x7FFFu + ((u >> 16) & 1u);
  return (u16)(u >> 16);
}
__device__ __forceinline__ float rbf(float f) { return bf2f(f2bf(f)); }
typedef float f32x2 __attribute__((ext_vector_type(2)));
typedef __bf16 bf16x2_t __attribute__((ext_vector_type(2)));
__device__ __forceinline__ unsigned pk2(float lo, float hi) {
  f32x2 v = {lo, hi};
  bf16x2_t b = __builtin_convertvector(v, bf16x2_t);
  return *reinterpret_cast<unsigned*>(&b);
}
__host__ __device__ constexpr int crow(int r, int hi) { return (r & 3) + 8 * (r >> 2) + 4 * hi; }

#define GLDS16(gptr, ldsptr) \
  __builtin_amdgcn_global_load_lds((const __attribute__((address_space(1))) unsigned*)(gptr), \
                                   (__attribute__((address_space(3))) unsigned*)(ldsptr), 16, 0, 0)

__device__ __forceinline__ void glds_asm(const void* g, unsigned lds) {
  asm volatile("s_mov_b32 m0, %1\n\ts_nop 0\n\tglobal_load_lds_dwordx4 %0, off" :: "v"(g), "s"(lds) : "memory");
}

__global__ __launch_bounds__(256) void ln_kernel(const u16* __restrict__ x, u16* __restrict__ xn, unsigned* __restrict__ cnt) {
  const int wave = threadIdx.x >> 6, lane = threadIdx.x & 63;
  const int row = blockIdx.x * 4 + wave;
  if (blockIdx.x < 2) cnt[blockIdx.x * 256 + threadIdx.x] = 0;
  float v[8];
  const bool act = lane < 56;
  if (act) {
    u32x4 w = *reinterpret_cast<const u32x4*>(x + (size_t)row * M_ + lane * 8);
#pragma unroll
    for (int i = 0; i < 4; ++i) { v[2 * i] = __uint_as_float(w[i] << 16); v[2 * i + 1] = __uint_as_float(w[i] & 0xffff0000u); }
  } else {
#pragma unroll
    for (int i = 0; i < 8; ++i) v[i] = 0.f;
  }
  float s = 0.f;
#pragma unroll
  for (int i = 0; i < 8; ++i) s += v[i];
#pragma unroll
  for (int o = 32; o >= 1; o >>= 1) s += __shfl_xor(s, o);
  const float mean = s * (1.f / M_);
  float q = 0.f;
  if (act) {
#pragma unroll
    for (int i = 0; i < 8; ++i) { float d = v[i] - mean; q += d * d; }
  }
#pragma unroll
  for (int o = 32; o >= 1; o >>= 1) q += __shfl_xor(q, o);
  const float var = q * (1.f / M_);
  const float mb = rbf(mean), vb = rbf(var);
  const float den = rbf(sqrtf(rbf(vb + 1e-5f)));
  if (act) {
    u32x4 o;
#pragma unroll
    for (int i = 0; i < 4; ++i) {
      float a = rbf(rbf(v[2 * i] - mb) / den), b = rbf(rbf(v[2 * i + 1] - mb) / den);
      o[i] = (unsigned)f2bf(a) | ((unsigned)f2bf(b) << 16);
    }
    *reinterpret_cast<u32x4*>(xn + (size_t)row * 512 + lane * 8) = o;
  } else {
    *reinterpret_cast<u32x4*>(xn + (size_t)row * 512 + lane * 8) = u32x4{0u, 0u, 0u, 0u};
  }
}

__device__ __forceinline__ unsigned offB(unsigned row, unsigned ch) { return 256u * row + 16u * (ch ^ (((row & 3) << 2) | ((row >> 2) & 3))); }
__device__ __forceinline__ int ldsA_byte(int r, int c) {
  int st = (r >> 4) * 2 + (c >> 5), rr = r & 15, cc = c & 31, ob = rr * 64 + cc * 2;
  return st * 1024 + (ob ^ (((ob >> 9) & 1) << 5));
}
__device__ __forceinline__ void stageA_rc(int b, int& R, int& C) {
  int st = b / 1024, sb = b % 1024, swz = sb ^ (((sb >> 9) & 1) << 5);
  R = (st >> 1) * 16 + swz / 64; C = (st & 1) * 32 + (swz % 64) / 2;
}

template <int MODE>
__device__ __forceinline__ void gemm256(const u16* __restrict__ A, int lda, const u16* __restrict__ B, int ldb, int nt, int kmax,
                                        int brow, int bcol, char* shm, u16* __restrict__ out, const u16* __restrict__ bias) {
  constexpr int HALF = 128, HTB = 16384;
#define SA(b, h) (shm + ((b) * 2 + (h)) * HTB)
#define SB(b, h) (shm + (4 + (b) * 2 + (h)) * HTB)
#define STAGE_A(T, P, br, kt) do { for (int _i = 0; _i < 2; ++_i) { int _b = (T) * 16 + _i * 8192; int _r, _c; stageA_rc(_b, _r, _c); \
      GLDS16(A + (size_t)((br) + _r) * lda + (kt) * 64 + _c, (P) + _b); } } while (0)
#define STAGE_B(T, P, bc, kt) do { for (int _i = 0; _i < 2; ++_i) { int _b = (T) * 16 + _i * 8192; int _r = _b >> 8; \
      int _ch = ((_b >> 4) & 15) ^ (((_r & 3) << 2) | ((_r >> 2) & 3)); int _k = (kt) * 64 + _r; if (MODE >= 2) _k = min(_k, kmax); \
      GLDS16(B + (size_t)_k * ldb + (bc) + _ch * 8, (P) + _b); } } while (0)
#define LDA(dst, b, h) for (int m = 0; m < 4; ++m) for (int k = 0; k < 2; ++k) \
    dst[m][k] = *reinterpret_cast<const bf16x8*>(SA(b, h) + ldsA_byte(wr * 64 + m * 16 + fr, k * 32 + fq * 8))
#define LDB(dst, b, h) for (int n = 0; n < 2; ++n) for (int k = 0; k < 2; ++k) { \
    const unsigned _a0 = offB(32 * k + 8 * fq + (fr >> 2), 2 * (wc * 2 + n) + ((lane & 3) >> 1)) + 8 * (lane & 1); \
    const unsigned _a1 = offB(32 * k + 8 * fq + 4 + (fr >> 2), 2 * (wc * 2 + n) + ((lane & 3) >> 1)) + 8 * (lane & 1); \
    s16x4 _lo = __builtin_amdgcn_ds_read_tr16_b64_v4i16((lds_s16x4*)(SB(b, h) + _a0)); \
    s16x4 _hi = __builtin_amdgcn_ds_read_tr16_b64_v4i16((lds_s16x4*)(SB(b, h) + _a1)); \
    dst[n][k] = bf16x8{_lo[0], _lo[1], _lo[2], _lo[3], _hi[0], _hi[1], _hi[2], _hi[3]}; }
#define MMA(ai, bj, At_, Bt_) do { __builtin_amdgcn_s_setprio(1); \
    for (int m = 0; m < 4; ++m) for (int n = 0; n < 2; ++n) for (int k = 0; k < 2; ++k) { \
      if (MODE == 1) acc[ai][bj][m][n] = __builtin_amdgcn_mfma_f32_16x16x32_bf16(At_[m][k], Bt_[n][k], acc[ai][bj][m][n], 0, 0, 0); \
      else           acc[ai][bj][m][n] = __builtin_amdgcn_mfma_f32_16x16x32_bf16(Bt_[n][k], At_[m][k], acc[ai][bj][m][n], 0, 0, 0); } \
    __builtin_amdgcn_s_setprio(0); } while (0)
#define WAIT_V(n) asm volatile("s_waitcnt vmcnt(" #n ")" ::: "memory")
#define WAIT_L(n) asm volatile("s_waitcnt lgkmcnt(" #n ")" ::: "memory")
#define BAR __builtin_amdgcn_s_barrier()
#define SCHED __builtin_amdgcn_sched_barrier(0)
  int wid = threadIdx.x >> 6, lane = threadIdx.x & 63, wr = wid >> 2, wc = wid & 3, fr = lane & 15, fq = lane >> 4;
  f32x4 acc[2][2][4][2] = {};
  bf16x8 At[4][2], B0[2][2], B1[2][2];
  const int tx = threadIdx.x;
  STAGE_B(tx, SB(0, 0), bcol, 0); STAGE_A(tx, SA(0, 0), brow, 0);
  STAGE_B(tx, SB(0, 1), bcol + HALF, 0); STAGE_A(tx, SA(0, 1), brow + HALF, 0);
  if (wr == 1) BAR;
  WAIT_V(4); BAR;
  STAGE_B(tx, SB(1, 0), bcol, 1); STAGE_A(tx, SA(1, 0), brow, 1); STAGE_B(tx, SB(1, 1), bcol + HALF, 1);
  WAIT_V(6); BAR;
  for (int t = 0; t < nt - 2; t += 2) {
    LDB(B0, 0, 0); SCHED; LDA(At, 0, 0); STAGE_A(tx, SA(1, 1), brow + HALF, t + 1);
    WAIT_L(8); BAR; WAIT_L(0); MMA(0, 0, At, B0); BAR; SCHED;
    LDB(B1, 0, 1); STAGE_B(tx, SB(0, 0), bcol, t + 2);
    BAR; WAIT_L(0); MMA(0, 1, At, B1); BAR;
    LDA(At, 0, 1); STAGE_A(tx, SA(0, 0), brow, t + 2);
    BAR; WAIT_L(0); MMA(1, 0, At, B0); BAR; SCHED;
    STAGE_B(tx, SB(0, 1), bcol + HALF, t + 2);
    WAIT_V(6); BAR; MMA(1, 1, At, B1); BAR;
    LDB(B0, 1, 0); SCHED; LDA(At, 1, 0); STAGE_A(tx, SA(0, 1), brow + HALF, t + 2);
    WAIT_L(8); BAR; WAIT_L(0); MMA(0, 0, At, B0); BAR; SCHED;
    LDB(B1, 1, 1); STAGE_B(tx, SB(1, 0), bcol, t + 3);
    BAR; WAIT_L(0); MMA(0, 1, At, B1); BAR;
    LDA(At, 1, 1); STAGE_A(tx, SA(1, 0), brow, t + 3);
    BAR; WAIT_L(0); MMA(1, 0, At, B0); BAR; SCHED;
    STAGE_B(tx, SB(1, 1), bcol + HALF, t + 3);
    WAIT_V(6); BAR; MMA(1, 1, At, B1); BAR;
  }
  int tz = threadIdx.x; asm volatile("" : "+v"(tz)); wid = tz >> 6; lane = tz & 63; wr = wid >> 2; wc = wid & 3; fr = lane & 15; fq = lane >> 4;
  { LDB(B0, 0, 0); WAIT_V(0); LDA(At, 0, 0); STAGE_A(tz, SA(1, 1), brow + HALF, nt - 1);
    BAR; WAIT_L(0); MMA(0, 0, At, B0); BAR;
    LDB(B1, 0, 1); BAR; WAIT_L(0); MMA(0, 1, At, B1); BAR;
    LDA(At, 0, 1); WAIT_V(4); BAR; WAIT_L(0); MMA(1, 0, At, B0); MMA(1, 1, At, B1); BAR; }
  { LDB(B0, 1, 0); LDA(At, 1, 0); WAIT_V(2); BAR; WAIT_L(0); MMA(0, 0, At, B0); BAR;
    LDB(B1, 1, 1); WAIT_V(0); BAR; WAIT_L(0); MMA(0, 1, At, B1); BAR;
    LDA(At, 1, 1); BAR; WAIT_L(0); MMA(1, 0, At, B0); MMA(1, 1, At, B1); BAR; }
  if (wr == 0) BAR;
#pragma unroll
  for (int ai = 0; ai < 2; ++ai)
#pragma unroll
  for (int bj = 0; bj < 2; ++bj)
#pragma unroll
  for (int mi = 0; mi < 4; ++mi)
#pragma unroll
  for (int ni = 0; ni < 2; ++ni) {
      const f32x4 v = acc[ai][bj][mi][ni];
      const int mb = brow + ai * HALF + wr * 64 + mi * 16, nb = bcol + bj * HALF + wc * 32 + ni * 16;
      if (MODE == 1) {
        const int n = nb + fr, m = mb + fq * 4;
        const int h = n >> 8, d = n & 255, tile = m >> 6, kk = m & 63;
        const int ks = kk >> 4, qq = (kk >> 2) & 3;
        const int ch = ks * 2 + (qq & 1), sub = qq >> 1;
        const size_t off = (((size_t)(h * 32 + tile) * 256 + d) * 64) + ((ch ^ ((d >> 1) & 7)) << 3) + sub * 4;
        u32x2 w = {pk2(v[0], v[1]), pk2(v[2], v[3])};
        *reinterpret_cast<u32x2*>(out + off) = w;
      } else {
        const int m = mb + fr, n = nb + fq * 4;
        if (MODE == 0) {
          const int h = n >> 8, d = n & 255, ch = d >> 3;
          const size_t off = ((size_t)h * S_ + m) * 256 + ((ch ^ (m & 15)) << 3) + (d & 7);
          u32x2 w = {pk2(v[0], v[1]), pk2(v[2], v[3])};
          *reinterpret_cast<u32x2*>(out + off) = w;
        } else if (MODE == 2) {
          constexpr float QS = 0.0625f * 1.4426950408889634f;
          u32x2 w = {pk2(v[0] * QS, v[1] * QS), pk2(v[2] * QS, v[3] * QS)};
          const int h = n >> 8, d = n & 255;
          const size_t off = ((size_t)((m >> 5) * 16 + h) * 16 + (d >> 4)) * 512 + (((d >> 3) & 1) * 32 + (m & 31)) * 8 + (d & 7);
          *reinterpret_cast<u32x2*>(out + off) = w;
        } else {
          float r[4];
          const u32x2 bw = *reinterpret_cast<const u32x2*>(bias + n);
#pragma unroll
          for (int j = 0; j < 4; ++j) {
            const float bv = (j & 1) ? __uint_as_float(bw[j >> 1] & 0xffff0000u) : __uint_as_float(bw[j >> 1] << 16);
            const float hh = rbf(rbf(v[j]) + bv);
            const float e = __expf(fminf(hh, 30.f));
            const float nn = e * (e + 2.f);
            r[j] = hh * (nn / (nn + 2.f));
          }
          u32x2 w = {pk2(r[0], r[1]), pk2(r[2], r[3])};
          *reinterpret_cast<u32x2*>(out + (size_t)m * F_ + n) = w;
        }
      }
    }
#undef SA
#undef SB
#undef STAGE_A
#undef STAGE_B
#undef LDA
#undef LDB
#undef MMA
}

__global__ __launch_bounds__(512, 2) void proj_kernel(const u16* __restrict__ xn, const u16* __restrict__ ckv,
                                                      const u16* __restrict__ wq, const u16* __restrict__ wuk,
                                                      const u16* __restrict__ wuv, const u16* __restrict__ w1,
                                                      const u16* __restrict__ b1, char* __restrict__ ws) {
  extern __shared__ __attribute__((aligned(16))) char smem[];
  int b = blockIdx.x;
  if (b < 256) {
    const int x = b & 7, i = b >> 3;
    const int ct = x * 4 + (i >> 3), rt = i & 7;
    if (ct < 16) gemm256<0>(ckv, DC_, wuk, HD_, DC_ / 64, DC_ - 1, rt * 256, ct * 256, smem, (u16*)(ws + WS_K), nullptr);
    else         gemm256<1>(ckv, DC_, wuv, HD_, DC_ / 64, DC_ - 1, rt * 256, (ct - 16) * 256, smem, (u16*)(ws + WS_VT), nullptr);
  } else if (b < 384) {
    b -= 256;
    const int x = b & 7, i = b >> 3;
    const int ct = x * 2 + (i >> 3), rt = i & 7;
    gemm256<2>(xn, 512, wq, HD_, 8, M_ - 1, rt * 256, ct * 256, smem, (u16*)(ws + WS_Q), nullptr);
  } else {
    b -= 384;
    gemm256<3>(xn, 512, w1, F_, 8, M_ - 1, (b & 7) * 256, (b >> 3) * 256, smem, (u16*)(ws + WS_ACT), b1);
  }
}

constexpr float SC2 = 0.0625f * 1.4426950408889634f;
constexpr float THR_LOG2 = 8.f;
#define ACLOB "a0","a1","a2","a3","a4","a5","a6","a7","a8","a9","a10","a11","a12","a13","a14","a15","a16","a17","a18","a19","a20","a21","a22","a23","a24","a25","a26","a27","a28","a29","a30","a31","a32","a33","a34","a35","a36","a37","a38","a39","a40","a41","a42","a43","a44","a45","a46","a47","a48","a49","a50","a51","a52","a53","a54","a55","a56","a57","a58","a59","a60","a61","a62","a63","a64","a65","a66","a67","a68","a69","a70","a71","a72","a73","a74","a75","a76","a77","a78","a79","a80","a81","a82","a83","a84","a85","a86","a87","a88","a89","a90","a91","a92","a93","a94","a95","a96","a97","a98","a99","a100","a101","a102","a103","a104","a105","a106","a107","a108","a109","a110","a111","a112","a113","a114","a115","a116","a117","a118","a119","a120","a121","a122","a123","a124","a125","a126","a127","a128","a129","a130","a131","a132","a133","a134","a135","a136","a137","a138","a139","a140","a141","a142","a143","a144","a145","a146","a147","a148","a149","a150","a151","a152","a153","a154","a155","a156","a157","a158","a159","a160","a161","a162","a163","a164","a165","a166","a167","a168","a169","a170","a171","a172","a173","a174","a175","a176","a177","a178","a179","a180","a181","a182","a183","a184","a185","a186","a187","a188","a189","a190","a191"
#define GAP() __builtin_amdgcn_sched_barrier(0)
template <int... Is> using iseq = std::integer_sequence<int, Is...>;
template <int N> using make_iseq = std::make_integer_sequence<int, N>;

#define TSPLIT 19
#define PV_KSMAJOR
#define PD 8
struct AW {
  f32x16 S[2][2];
  u32x4 P[4];
  bf16x8 kf[32];
  bf16x8 vf[32];
  float pe[32];
  f32x16 negm;
  float m, l, rs0, rs1, pm0, pm1, pmax, ninf;
  unsigned ka[8];
  unsigned va[4];
  unsigned voff;
  unsigned sk, sv;
  unsigned m0b;
  __amdgpu_buffer_rsrc_t srd;
  char* smem; int wid;
  int thr, q, hi;
};

template <int OFF> __device__ __forceinline__ void ds_ld(bf16x8& dst, unsigned addr) {
  asm volatile("ds_read_b128 %0, %1 offset:%c2" : "=v"(dst) : "v"(addr), "i"(OFF) : "memory");
}
template <int X, int M> constexpr int k_off() { return ((M >> 1) >> 3) * 256 + (M & 1) * 16384 + X * 32768; }
constexpr int pv_d(int M) { return M & 7; }
constexpr int pv_ks(int M) { return M >> 3; }
template <int X, int M> constexpr int v_off() { return pv_d(M) * 4096 + X * 32768; }
template <int X, int M> __device__ __forceinline__ void k_load(AW& w) { ds_ld<k_off<X, M>()>(w.kf[M], w.ka[(M >> 1) & 7]); }
template <int X, int M> __device__ __forceinline__ void v_load(AW& w) { ds_ld<v_off<X, M>()>(w.vf[M], w.va[pv_ks(M)]); }
template <int X, int... Ms> __device__ __forceinline__ void k_pre(AW& w, iseq<Ms...>) { (k_load<X, Ms>(w), ...); }
template <int X, int M, int W, bool FIRST = false> __device__ __forceinline__ void qk_mfma(AW& w) {
  constexpr int st = M >> 1, half = M & 1;
  if constexpr (W >= 0) asm volatile("s_waitcnt lgkmcnt(%c0)" :: "i"(W) : "memory");
  if constexpr (st == 0 && FIRST)
    asm volatile("v_mfma_f32_32x32x16_bf16 %0, %1, a[%c2:%c3], 0" : "=&v"(w.S[X][half]) : "v"(w.kf[M]), "i"(128 + 4 * st), "i"(128 + 4 * st + 3) : ACLOB);
  else if constexpr (st == 0)
    asm volatile("v_mfma_f32_32x32x16_bf16 %0, %1, a[%c2:%c3], %4" : "=&v"(w.S[X][half]) : "v"(w.kf[M]), "i"(128 + 4 * st), "i"(128 + 4 * st + 3), "v"(w.negm) : ACLOB);
  else
    asm volatile("v_mfma_f32_32x32x16_bf16 %0, %1, a[%c2:%c3], %0" : "+v"(w.S[X][half]) : "v"(w.kf[M]), "i"(128 + 4 * st), "i"(128 + 4 * st + 3) : ACLOB);
}
template <int M, int W> __device__ __forceinline__ void pv_mfma(AW& w) {
  constexpr int d = pv_d(M), ks = pv_ks(M);
  if constexpr (W >= 0) asm volatile("s_waitcnt lgkmcnt(%c0)" :: "i"(W) : "memory");
  asm volatile("v_mfma_f32_32x32x16_bf16 a[%c2:%c3], %0, %1, a[%c2:%c3]" :: "v"(w.vf[M]), "v"(w.P[ks]), "i"(16 * d), "i"(16 * d + 15) : ACLOB);
}
template <int M> constexpr int wcnt() { return (M & 1) ? -1 : ((M + PD <= 32) ? PD - 2 : (32 - M - 2 > 0 ? 32 - M - 2 : 0)); }
template <int XC, int M> __device__ __forceinline__ void sm_gap(AW& w) {
  const float sv = w.S[XC][M >> 4][M & 15];
  if constexpr (M == 0) {
    asm volatile("v_exp_f32 %0, %1" : "=&v"(w.pe[0]) : "v"(sv));
  } else if constexpr ((M - 1) & 1) {
    constexpr int E = M - 1, ks = E >> 3, dw = (E >> 1) & 3;
    unsigned pk;
    asm volatile("v_exp_f32 %0, %3\n\tv_add_f32 %1, %1, %5\n\tv_cvt_pk_bf16_f32 %2, %4, %5"
                 : "=&v"(w.pe[M]), "+v"(w.rs1), "=&v"(pk) : "v"(sv), "v"(w.pe[E - 1]), "v"(w.pe[E]));
    w.P[ks][dw] = pk;
  } else {
    asm volatile("v_exp_f32 %0, %2\n\tv_add_f32 %1, %1, %3"
                 : "=&v"(w.pe[M]), "+v"(w.rs0) : "v"(sv), "v"(w.pe[M - 1]));
  }
}
template <int XC, bool DOQK, int M> __device__ __forceinline__ void gapA(AW& w) {
  constexpr int XN = XC ^ 1;
  if constexpr (DOQK) {
    qk_mfma<XN, M, (M & 1) ? -1 : PD - 2>(w);
    if constexpr (M + PD < 32) k_load<XN, M + PD>(w);
  }
  if constexpr (M >= 32 - PD) v_load<XC, M - (32 - PD)>(w);
  sm_gap<XC, M>(w);
  GAP();
}
template <int XC, bool DOQK, bool MASK, int M> __device__ __forceinline__ void gapB(AW& w) {
  constexpr int XN = XC ^ 1;
  pv_mfma<M, wcnt<M>()>(w);
  if constexpr (M + PD < 32) v_load<XC, M + PD>(w);
  if constexpr (DOQK) {
    if constexpr (M < 16) {
      constexpr int g = (M >> 2) & 1;
      constexpr unsigned ldsb = (M < 8 ? 65536u + XN * 32768u : XC * 32768u) + g * 4096u;
      const unsigned soff = (M < 8 ? w.sv : w.sk) + g * 4096u;
      if constexpr ((M & 3) == 0)
        asm volatile("s_mov_b32 m0, %0\n\ts_nop 0\n\tbuffer_load_dwordx4 %1, %2, %3 offen lds"
                     :: "s"(w.m0b + ldsb), "v"(w.voff), "s"(w.srd), "s"(soff) : "memory");
      else
        asm volatile("buffer_load_dwordx4 %0, %1, %2 offen offset:%c3 lds"
                     :: "v"(w.voff), "s"(w.srd), "s"(soff), "i"((M & 3) * 1024) : "memory");
    }
    if constexpr (M >= 2 && M < 18) {
      constexpr int r = M - 2;
      float a = w.S[XN][0][r], b = w.S[XN][1][r];
      float& pm = (r & 1) ? w.pm1 : w.pm0;
      if constexpr (MASK) {
        asm volatile("v_cmp_gt_i32 vcc, %c5, %3\n\tv_cndmask_b32 %0, %0, %4, vcc\n\tv_cmp_gt_i32 vcc, %c6, %3\n\tv_cndmask_b32 %1, %1, %4, vcc\n\tv_max3_f32 %2, %2, %0, %1"
                     : "+v"(a), "+v"(b), "+v"(pm) : "v"(w.thr), "v"(w.ninf), "i"(crow(r, 0)), "i"(crow(r, 0) + 32) : "vcc");
        w.S[XN][0][r] = a; w.S[XN][1][r] = b;
      } else {
        asm volatile("v_max3_f32 %0, %0, %1, %2" : "+v"(pm) : "v"(a), "v"(b));
      }
    }
  }
  GAP();
}
template <int XN> __device__ __forceinline__ void attn_decide(AW& w) {
  if (__builtin_expect(!__all(w.pmax <= THR_LOG2), 0)) {
    const float dl = fmaxf(w.pmax, 0.f);
    const float alpha = __builtin_amdgcn_exp2f(-dl);
    w.m += dl; w.l *= alpha;
#pragma unroll
    for (int r = 0; r < 16; ++r) { w.S[XN][0][r] -= dl; w.S[XN][1][r] -= dl; w.negm[r] = -w.m; }
    float t0, t1;
    asm volatile("s_nop 7\n\ts_nop 7\n\t"
    "v_accvgpr_read_b32 %0, a0\n\tv_mul_f32 %0, %2, %0\n\tv_accvgpr_write_b32 a0, %0\n\t"
    "v_accvgpr_read_b32 %1, a1\n\tv_mul_f32 %1, %2, %1\n\tv_accvgpr_write_b32 a1, %1\n\t"
    "v_accvgpr_read_b32 %0, a2\n\tv_mul_f32 %0, %2, %0\n\tv_accvgpr_write_b32 a2, %0\n\t"
    "v_accvgpr_read_b32 %1, a3\n\tv_mul_f32 %1, %2, %1\n\tv_accvgpr_write_b32 a3, %1\n\t"
    "v_accvgpr_read_b32 %0, a4\n\tv_mul_f32 %0, %2, %0\n\tv_accvgpr_write_b32 a4, %0\n\t"
    "v_accvgpr_read_b32 %1, a5\n\tv_mul_f32 %1, %2, %1\n\tv_accvgpr_write_b32 a5, %1\n\t"
    "v_accvgpr_read_b32 %0, a6\n\tv_mul_f32 %0, %2, %0\n\tv_accvgpr_write_b32 a6, %0\n\t"
    "v_accvgpr_read_b32 %1, a7\n\tv_mul_f32 %1, %2, %1\n\tv_accvgpr_write_b32 a7, %1\n\t"
    "v_accvgpr_read_b32 %0, a8\n\tv_mul_f32 %0, %2, %0\n\tv_accvgpr_write_b32 a8, %0\n\t"
    "v_accvgpr_read_b32 %1, a9\n\tv_mul_f32 %1, %2, %1\n\tv_accvgpr_write_b32 a9, %1\n\t"
    "v_accvgpr_read_b32 %0, a10\n\tv_mul_f32 %0, %2, %0\n\tv_accvgpr_write_b32 a10, %0\n\t"
    "v_accvgpr_read_b32 %1, a11\n\tv_mul_f32 %1, %2, %1\n\tv_accvgpr_write_b32 a11, %1\n\t"
    "v_accvgpr_read_b32 %0, a12\n\tv_mul_f32 %0, %2, %0\n\tv_accvgpr_write_b32 a12, %0\n\t"
    "v_accvgpr_read_b32 %1, a13\n\tv_mul_f32 %1, %2, %1\n\tv_accvgpr_write_b32 a13, %1\n\t"
    "v_accvgpr_read_b32 %0, a14\n\tv_mul_f32 %0, %2, %0\n\tv_accvgpr_write_b32 a14, %0\n\t"
    "v_accvgpr_read_b32 %1, a15\n\tv_mul_f32 %1, %2, %1\n\tv_accvgpr_write_b32 a15, %1\n\t"
    "v_accvgpr_read_b32 %0, a16\n\tv_mul_f32 %0, %2, %0\n\tv_accvgpr_write_b32 a16, %0\n\t"
    "v_accvgpr_read_b32 %1, a17\n\tv_mul_f32 %1, %2, %1\n\tv_accvgpr_write_b32 a17, %1\n\t"
    "v_accvgpr_read_b32 %0, a18\n\tv_mul_f32 %0, %2, %0\n\tv_accvgpr_write_b32 a18, %0\n\t"
    "v_accvgpr_read_b32 %1, a19\n\tv_mul_f32 %1, %2, %1\n\tv_accvgpr_write_b32 a19, %1\n\t"
    "v_accvgpr_read_b32 %0, a20\n\tv_mul_f32 %0, %2, %0\n\tv_accvgpr_write_b32 a20, %0\n\t"
    "v_accvgpr_read_b32 %1, a21\n\tv_mul_f32 %1, %2, %1\n\tv_accvgpr_write_b32 a21, %1\n\t"
    "v_accvgpr_read_b32 %0, a22\n\tv_mul_f32 %0, %2, %0\n\tv_accvgpr_write_b32 a22, %0\n\t"
    "v_accvgpr_read_b32 %1, a23\n\tv_mul_f32 %1, %2, %1\n\tv_accvgpr_write_b32 a23, %1\n\t"
    "v_accvgpr_read_b32 %0, a24\n\tv_mul_f32 %0, %2, %0\n\tv_accvgpr_write_b32 a24, %0\n\t"
    "v_accvgpr_read_b32 %1, a25\n\tv_mul_f32 %1, %2, %1\n\tv_accvgpr_write_b32 a25, %1\n\t"
    "v_accvgpr_read_b32 %0, a26\n\tv_mul_f32 %0, %2, %0\n\tv_accvgpr_write_b32 a26, %0\n\t"
    "v_accvgpr_read_b32 %1, a27\n\tv_mul_f32 %1, %2, %1\n\tv_accvgpr_write_b32 a27, %1\n\t"
    "v_accvgpr_read_b32 %0, a28\n\tv_mul_f32 %0, %2, %0\n\tv_accvgpr_write_b32 a28, %0\n\t"
    "v_accvgpr_read_b32 %1, a29\n\tv_mul_f32 %1, %2, %1\n\tv_accvgpr_write_b32 a29, %1\n\t"
    "v_accvgpr_read_b32 %0, a30\n\tv_mul_f32 %0, %2, %0\n\tv_accvgpr_write_b32 a30, %0\n\t"
    "v_accvgpr_read_b32 %1, a31\n\tv_mul_f32 %1, %2, %1\n\tv_accvgpr_write_b32 a31, %1\n\t"
    "v_accvgpr_read_b32 %0, a32\n\tv_mul_f32 %0, %2, %0\n\tv_accvgpr_write_b32 a32, %0\n\t"
    "v_accvgpr_read_b32 %1, a33\n\tv_mul_f32 %1, %2, %1\n\tv_accvgpr_write_b32 a33, %1\n\t"
    "v_accvgpr_read_b32 %0, a34\n\tv_mul_f32 %0, %2, %0\n\tv_accvgpr_write_b32 a34, %0\n\t"
    "v_accvgpr_read_b32 %1, a35\n\tv_mul_f32 %1, %2, %1\n\tv_accvgpr_write_b32 a35, %1\n\t"
    "v_accvgpr_read_b32 %0, a36\n\tv_mul_f32 %0, %2, %0\n\tv_accvgpr_write_b32 a36, %0\n\t"
    "v_accvgpr_read_b32 %1, a37\n\tv_mul_f32 %1, %2, %1\n\tv_accvgpr_write_b32 a37, %1\n\t"
    "v_accvgpr_read_b32 %0, a38\n\tv_mul_f32 %0, %2, %0\n\tv_accvgpr_write_b32 a38, %0\n\t"
    "v_accvgpr_read_b32 %1, a39\n\tv_mul_f32 %1, %2, %1\n\tv_accvgpr_write_b32 a39, %1\n\t"
    "v_accvgpr_read_b32 %0, a40\n\tv_mul_f32 %0, %2, %0\n\tv_accvgpr_write_b32 a40, %0\n\t"
    "v_accvgpr_read_b32 %1, a41\n\tv_mul_f32 %1, %2, %1\n\tv_accvgpr_write_b32 a41, %1\n\t"
    "v_accvgpr_read_b32 %0, a42\n\tv_mul_f32 %0, %2, %0\n\tv_accvgpr_write_b32 a42, %0\n\t"
    "v_accvgpr_read_b32 %1, a43\n\tv_mul_f32 %1, %2, %1\n\tv_accvgpr_write_b32 a43, %1\n\t"
    "v_accvgpr_read_b32 %0, a44\n\tv_mul_f32 %0, %2, %0\n\tv_accvgpr_write_b32 a44, %0\n\t"
    "v_accvgpr_read_b32 %1, a45\n\tv_mul_f32 %1, %2, %1\n\tv_accvgpr_write_b32 a45, %1\n\t"
    "v_accvgpr_read_b32 %0, a46\n\tv_mul_f32 %0, %2, %0\n\tv_accvgpr_write_b32 a46, %0\n\t"
    "v_accvgpr_read_b32 %1, a47\n\tv_mul_f32 %1, %2, %1\n\tv_accvgpr_write_b32 a47, %1\n\t"
    "v_accvgpr_read_b32 %0, a48\n\tv_mul_f32 %0, %2, %0\n\tv_accvgpr_write_b32 a48, %0\n\t"
    "v_accvgpr_read_b32 %1, a49\n\tv_mul_f32 %1, %2, %1\n\tv_accvgpr_write_b32 a49, %1\n\t"
    "v_accvgpr_read_b32 %0, a50\n\tv_mul_f32 %0, %2, %0\n\tv_accvgpr_write_b32 a50, %0\n\t"
    "v_accvgpr_read_b32 %1, a51\n\tv_mul_f32 %1, %2, %1\n\tv_accvgpr_write_b32 a51, %1\n\t"
    "v_accvgpr_read_b32 %0, a52\n\tv_mul_f32 %0, %2, %0\n\tv_accvgpr_write_b32 a52, %0\n\t"
    "v_accvgpr_read_b32 %1, a53\n\tv_mul_f32 %1, %2, %1\n\tv_accvgpr_write_b32 a53, %1\n\t"
    "v_accvgpr_read_b32 %0, a54\n\tv_mul_f32 %0, %2, %0\n\tv_accvgpr_write_b32 a54, %0\n\t"
    "v_accvgpr_read_b32 %1, a55\n\tv_mul_f32 %1, %2, %1\n\tv_accvgpr_write_b32 a55, %1\n\t"
    "v_accvgpr_read_b32 %0, a56\n\tv_mul_f32 %0, %2, %0\n\tv_accvgpr_write_b32 a56, %0\n\t"
    "v_accvgpr_read_b32 %1, a57\n\tv_mul_f32 %1, %2, %1\n\tv_accvgpr_write_b32 a57, %1\n\t"
    "v_accvgpr_read_b32 %0, a58\n\tv_mul_f32 %0, %2, %0\n\tv_accvgpr_write_b32 a58, %0\n\t"
    "v_accvgpr_read_b32 %1, a59\n\tv_mul_f32 %1, %2, %1\n\tv_accvgpr_write_b32 a59, %1\n\t"
    "v_accvgpr_read_b32 %0, a60\n\tv_mul_f32 %0, %2, %0\n\tv_accvgpr_write_b32 a60, %0\n\t"
    "v_accvgpr_read_b32 %1, a61\n\tv_mul_f32 %1, %2, %1\n\tv_accvgpr_write_b32 a61, %1\n\t"
    "v_accvgpr_read_b32 %0, a62\n\tv_mul_f32 %0, %2, %0\n\tv_accvgpr_write_b32 a62, %0\n\t"
    "v_accvgpr_read_b32 %1, a63\n\tv_mul_f32 %1, %2, %1\n\tv_accvgpr_write_b32 a63, %1\n\t"
    "v_accvgpr_read_b32 %0, a64\n\tv_mul_f32 %0, %2, %0\n\tv_accvgpr_write_b32 a64, %0\n\t"
    "v_accvgpr_read_b32 %1, a65\n\tv_mul_f32 %1, %2, %1\n\tv_accvgpr_write_b32 a65, %1\n\t"
    "v_accvgpr_read_b32 %0, a66\n\tv_mul_f32 %0, %2, %0\n\tv_accvgpr_write_b32 a66, %0\n\t"
    "v_accvgpr_read_b32 %1, a67\n\tv_mul_f32 %1, %2, %1\n\tv_accvgpr_write_b32 a67, %1\n\t"
    "v_accvgpr_read_b32 %0, a68\n\tv_mul_f32 %0, %2, %0\n\tv_accvgpr_write_b32 a68, %0\n\t"
    "v_accvgpr_read_b32 %1, a69\n\tv_mul_f32 %1, %2, %1\n\tv_accvgpr_write_b32 a69, %1\n\t"
    "v_accvgpr_read_b32 %0, a70\n\tv_mul_f32 %0, %2, %0\n\tv_accvgpr_write_b32 a70, %0\n\t"
    "v_accvgpr_read_b32 %1, a71\n\tv_mul_f32 %1, %2, %1\n\tv_accvgpr_write_b32 a71, %1\n\t"
    "v_accvgpr_read_b32 %0, a72\n\tv_mul_f32 %0, %2, %0\n\tv_accvgpr_write_b32 a72, %0\n\t"
    "v_accvgpr_read_b32 %1, a73\n\tv_mul_f32 %1, %2, %1\n\tv_accvgpr_write_b32 a73, %1\n\t"
    "v_accvgpr_read_b32 %0, a74\n\tv_mul_f32 %0, %2, %0\n\tv_accvgpr_write_b32 a74, %0\n\t"
    "v_accvgpr_read_b32 %1, a75\n\tv_mul_f32 %1, %2, %1\n\tv_accvgpr_write_b32 a75, %1\n\t"
    "v_accvgpr_read_b32 %0, a76\n\tv_mul_f32 %0, %2, %0\n\tv_accvgpr_write_b32 a76, %0\n\t"
    "v_accvgpr_read_b32 %1, a77\n\tv_mul_f32 %1, %2, %1\n\tv_accvgpr_write_b32 a77, %1\n\t"
    "v_accvgpr_read_b32 %0, a78\n\tv_mul_f32 %0, %2, %0\n\tv_accvgpr_write_b32 a78, %0\n\t"
    "v_accvgpr_read_b32 %1, a79\n\tv_mul_f32 %1, %2, %1\n\tv_accvgpr_write_b32 a79, %1\n\t"
    "v_accvgpr_read_b32 %0, a80\n\tv_mul_f32 %0, %2, %0\n\tv_accvgpr_write_b32 a80, %0\n\t"
    "v_accvgpr_read_b32 %1, a81\n\tv_mul_f32 %1, %2, %1\n\tv_accvgpr_write_b32 a81, %1\n\t"
    "v_accvgpr_read_b32 %0, a82\n\tv_mul_f32 %0, %2, %0\n\tv_accvgpr_write_b32 a82, %0\n\t"
    "v_accvgpr_read_b32 %1, a83\n\tv_mul_f32 %1, %2, %1\n\tv_accvgpr_write_b32 a83, %1\n\t"
    "v_accvgpr_read_b32 %0, a84\n\tv_mul_f32 %0, %2, %0\n\tv_accvgpr_write_b32 a84, %0\n\t"
    "v_accvgpr_read_b32 %1, a85\n\tv_mul_f32 %1, %2, %1\n\tv_accvgpr_write_b32 a85, %1\n\t"
    "v_accvgpr_read_b32 %0, a86\n\tv_mul_f32 %0, %2, %0\n\tv_accvgpr_write_b32 a86, %0\n\t"
    "v_accvgpr_read_b32 %1, a87\n\tv_mul_f32 %1, %2, %1\n\tv_accvgpr_write_b32 a87, %1\n\t"
    "v_accvgpr_read_b32 %0, a88\n\tv_mul_f32 %0, %2, %0\n\tv_accvgpr_write_b32 a88, %0\n\t"
    "v_accvgpr_read_b32 %1, a89\n\tv_mul_f32 %1, %2, %1\n\tv_accvgpr_write_b32 a89, %1\n\t"
    "v_accvgpr_read_b32 %0, a90\n\tv_mul_f32 %0, %2, %0\n\tv_accvgpr_write_b32 a90, %0\n\t"
    "v_accvgpr_read_b32 %1, a91\n\tv_mul_f32 %1, %2, %1\n\tv_accvgpr_write_b32 a91, %1\n\t"
    "v_accvgpr_read_b32 %0, a92\n\tv_mul_f32 %0, %2, %0\n\tv_accvgpr_write_b32 a92, %0\n\t"
    "v_accvgpr_read_b32 %1, a93\n\tv_mul_f32 %1, %2, %1\n\tv_accvgpr_write_b32 a93, %1\n\t"
    "v_accvgpr_read_b32 %0, a94\n\tv_mul_f32 %0, %2, %0\n\tv_accvgpr_write_b32 a94, %0\n\t"
    "v_accvgpr_read_b32 %1, a95\n\tv_mul_f32 %1, %2, %1\n\tv_accvgpr_write_b32 a95, %1\n\t"
    "v_accvgpr_read_b32 %0, a96\n\tv_mul_f32 %0, %2, %0\n\tv_accvgpr_write_b32 a96, %0\n\t"
    "v_accvgpr_read_b32 %1, a97\n\tv_mul_f32 %1, %2, %1\n\tv_accvgpr_write_b32 a97, %1\n\t"
    "v_accvgpr_read_b32 %0, a98\n\tv_mul_f32 %0, %2, %0\n\tv_accvgpr_write_b32 a98, %0\n\t"
    "v_accvgpr_read_b32 %1, a99\n\tv_mul_f32 %1, %2, %1\n\tv_accvgpr_write_b32 a99, %1\n\t"
    "v_accvgpr_read_b32 %0, a100\n\tv_mul_f32 %0, %2, %0\n\tv_accvgpr_write_b32 a100, %0\n\t"
    "v_accvgpr_read_b32 %1, a101\n\tv_mul_f32 %1, %2, %1\n\tv_accvgpr_write_b32 a101, %1\n\t"
    "v_accvgpr_read_b32 %0, a102\n\tv_mul_f32 %0, %2, %0\n\tv_accvgpr_write_b32 a102, %0\n\t"
    "v_accvgpr_read_b32 %1, a103\n\tv_mul_f32 %1, %2, %1\n\tv_accvgpr_write_b32 a103, %1\n\t"
    "v_accvgpr_read_b32 %0, a104\n\tv_mul_f32 %0, %2, %0\n\tv_accvgpr_write_b32 a104, %0\n\t"
    "v_accvgpr_read_b32 %1, a105\n\tv_mul_f32 %1, %2, %1\n\tv_accvgpr_write_b32 a105, %1\n\t"
    "v_accvgpr_read_b32 %0, a106\n\tv_mul_f32 %0, %2, %0\n\tv_accvgpr_write_b32 a106, %0\n\t"
    "v_accvgpr_read_b32 %1, a107\n\tv_mul_f32 %1, %2, %1\n\tv_accvgpr_write_b32 a107, %1\n\t"
    "v_accvgpr_read_b32 %0, a108\n\tv_mul_f32 %0, %2, %0\n\tv_accvgpr_write_b32 a108, %0\n\t"
    "v_accvgpr_read_b32 %1, a109\n\tv_mul_f32 %1, %2, %1\n\tv_accvgpr_write_b32 a109, %1\n\t"
    "v_accvgpr_read_b32 %0, a110\n\tv_mul_f32 %0, %2, %0\n\tv_accvgpr_write_b32 a110, %0\n\t"
    "v_accvgpr_read_b32 %1, a111\n\tv_mul_f32 %1, %2, %1\n\tv_accvgpr_write_b32 a111, %1\n\t"
    "v_accvgpr_read_b32 %0, a112\n\tv_mul_f32 %0, %2, %0\n\tv_accvgpr_write_b32 a112, %0\n\t"
    "v_accvgpr_read_b32 %1, a113\n\tv_mul_f32 %1, %2, %1\n\tv_accvgpr_write_b32 a113, %1\n\t"
    "v_accvgpr_read_b32 %0, a114\n\tv_mul_f32 %0, %2, %0\n\tv_accvgpr_write_b32 a114, %0\n\t"
    "v_accvgpr_read_b32 %1, a115\n\tv_mul_f32 %1, %2, %1\n\tv_accvgpr_write_b32 a115, %1\n\t"
    "v_accvgpr_read_b32 %0, a116\n\tv_mul_f32 %0, %2, %0\n\tv_accvgpr_write_b32 a116, %0\n\t"
    "v_accvgpr_read_b32 %1, a117\n\tv_mul_f32 %1, %2, %1\n\tv_accvgpr_write_b32 a117, %1\n\t"
    "v_accvgpr_read_b32 %0, a118\n\tv_mul_f32 %0, %2, %0\n\tv_accvgpr_write_b32 a118, %0\n\t"
    "v_accvgpr_read_b32 %1, a119\n\tv_mul_f32 %1, %2, %1\n\tv_accvgpr_write_b32 a119, %1\n\t"
    "v_accvgpr_read_b32 %0, a120\n\tv_mul_f32 %0, %2, %0\n\tv_accvgpr_write_b32 a120, %0\n\t"
    "v_accvgpr_read_b32 %1, a121\n\tv_mul_f32 %1, %2, %1\n\tv_accvgpr_write_b32 a121, %1\n\t"
    "v_accvgpr_read_b32 %0, a122\n\tv_mul_f32 %0, %2, %0\n\tv_accvgpr_write_b32 a122, %0\n\t"
    "v_accvgpr_read_b32 %1, a123\n\tv_mul_f32 %1, %2, %1\n\tv_accvgpr_write_b32 a123, %1\n\t"
    "v_accvgpr_read_b32 %0, a124\n\tv_mul_f32 %0, %2, %0\n\tv_accvgpr_write_b32 a124, %0\n\t"
    "v_accvgpr_read_b32 %1, a125\n\tv_mul_f32 %1, %2, %1\n\tv_accvgpr_write_b32 a125, %1\n\t"
    "v_accvgpr_read_b32 %0, a126\n\tv_mul_f32 %0, %2, %0\n\tv_accvgpr_write_b32 a126, %0\n\t"
    "v_accvgpr_read_b32 %1, a127\n\tv_mul_f32 %1, %2, %1\n\tv_accvgpr_write_b32 a127, %1\n\t"
    "s_nop 1" : "=&v"(t0), "=&v"(t1) : "v"(alpha) : ACLOB);
  }
}
#define STAMP()
#define STAMP3()
#define STAMP2()
template <int XC, bool DOQK, bool MASK, int... Ms> __device__ __forceinline__ void attn_step_(AW& w, iseq<Ms...>) {
  constexpr int XN = XC ^ 1;
  w.rs0 = 0.f; w.rs1 = 0.f; w.pm0 = w.ninf; w.pm1 = w.ninf;
  asm volatile("" : "+v"(w.rs0), "+v"(w.rs1), "+v"(w.pm0), "+v"(w.pm1));
  if constexpr (DOQK) k_pre<XN>(w, make_iseq<PD>{});
  STAMP2();
  GAP();
  (gapA<XC, DOQK, Ms>(w), ...);
  { unsigned pk;
    asm volatile("s_nop 0\n\tv_add_f32 %0, %0, %3\n\tv_cvt_pk_bf16_f32 %1, %2, %3" : "+v"(w.rs1), "=&v"(pk) : "v"(w.pe[30]), "v"(w.pe[31]));
    w.P[3][3] = pk; }
  { float ps = w.rs0 + w.rs1;
    auto rr = __builtin_amdgcn_permlane32_swap(__float_as_uint(ps), __float_as_uint(ps), false, false);
    w.l += __uint_as_float(rr[0]) + __uint_as_float(rr[1]); }
  asm volatile("s_nop 1" ::: "memory");
  STAMP2();
  GAP();
  (gapB<XC, DOQK, MASK, Ms>(w), ...);
  STAMP2();
  if constexpr (DOQK) {
    float a = fmaxf(w.pm0, w.pm1);
    auto rr = __builtin_amdgcn_permlane32_swap(__float_as_uint(a), __float_as_uint(a), false, false);
    w.pmax = fmaxf(__uint_as_float(rr[0]), __uint_as_float(rr[1]));
    attn_decide<XN>(w);
    w.sk += 32768; w.sv += 32768; w.thr -= 64;
    asm volatile("s_waitcnt vmcnt(0)" ::: "memory");
    __builtin_amdgcn_s_barrier();
    GAP();
  }
}
template <int XC, bool DOQK, bool MASK> __device__ __forceinline__ void attn_step(AW& w) { attn_step_<XC, DOQK, MASK>(w, make_iseq<32>{}); }

template <int M> __device__ __forceinline__ void qk_first1(AW& w) {
  qk_mfma<0, M, wcnt<M>(), true>(w);
  if constexpr (M + PD < 32) k_load<0, M + PD>(w);
}
template <int... Ms> __device__ __forceinline__ void qk_first(AW& w, iseq<Ms...>) {
  k_pre<0>(w, make_iseq<PD>{});
  (qk_first1<Ms>(w), ...);
}
template <int I> __device__ __forceinline__ void q_write1(const bf16x8& q) {
  const u32x4 t = __builtin_bit_cast(u32x4, q);
  asm volatile("v_accvgpr_write_b32 a[%c4], %0\n\tv_accvgpr_write_b32 a[%c5], %1\n\tv_accvgpr_write_b32 a[%c6], %2\n\tv_accvgpr_write_b32 a[%c7], %3"
               :: "v"(t[0]), "v"(t[1]), "v"(t[2]), "v"(t[3]), "i"(128 + 4 * I), "i"(129 + 4 * I), "i"(130 + 4 * I), "i"(131 + 4 * I) : ACLOB);
}
template <int... Is> __device__ __forceinline__ void q_load_all(const u16* qp, iseq<Is...>) {
  bf16x8 t[16];
  ((t[Is] = *reinterpret_cast<const bf16x8*>(qp + Is * 512)), ...);
  (q_write1<Is>(t[Is]), ...);
}
template <int I> __device__ __forceinline__ void o_zero1() { asm volatile("v_accvgpr_write_b32 a[%c0], 0" :: "i"(I) : ACLOB); }
template <int... Is> __device__ __forceinline__ void o_zero(iseq<Is...>) { (o_zero1<Is>(), ...); }
template <int I> __device__ __forceinline__ f32x4 o_read4() {
  f32x4 r;
  asm volatile("v_accvgpr_read_b32 %0, a[%c4]\n\tv_accvgpr_read_b32 %1, a[%c5]\n\tv_accvgpr_read_b32 %2, a[%c6]\n\tv_accvgpr_read_b32 %3, a[%c7]"
               : "=v"(r[0]), "=v"(r[1]), "=v"(r[2]), "=v"(r[3]) : "i"(4 * I), "i"(4 * I + 1), "i"(4 * I + 2), "i"(4 * I + 3) : ACLOB);
  return r;
}

__device__ __forceinline__ void attn_segment(AW& w, const u16* __restrict__ Qb, const u16* __restrict__ Kh,
                                             const u16* __restrict__ Vh, int h, int qb, int t0, int t1) {
  const int tid = threadIdx.x;
  const int q = qb * 128 + w.wid * 32 + (tid & 31);
  w.q = q;
  STAMP();
  __syncthreads();
  const int n = t1 - t0;
  {
    const u16* k0 = Kh + (size_t)t0 * 16384 + w.wid * 4096 + (tid & 63) * 8;
    const u16* v0 = Vh + (size_t)t0 * 16384 + w.wid * 4096 + (tid & 63) * 8;
#pragma unroll
    for (int i = 0; i < 8; ++i) GLDS16(k0 + i * 512, w.smem + i * 1024 + w.wid * 8192);
#pragma unroll
    for (int i = 0; i < 8; ++i) GLDS16(v0 + i * 512, w.smem + 65536 + i * 1024 + w.wid * 8192);
    if (n > 1) {
#pragma unroll
      for (int i = 0; i < 8; ++i) GLDS16(k0 + 16384 + i * 512, w.smem + 32768 + i * 1024 + w.wid * 8192);
    }
    w.sk = (unsigned)(h * S_ * 512 + (t0 + 2) * 32768);
    w.sv = (unsigned)(WS_VT - WS_K) + (unsigned)(h * S_ * 512 + (t0 + 1) * 32768);
  }
  q_load_all(Qb + (size_t)((q >> 5) * 16 + h) * 8192 + (tid & 63) * 8, make_iseq<16>{});
  o_zero(make_iseq<128>{});
  w.m = -1e30f; w.l = 0.f;
  asm volatile("s_waitcnt vmcnt(0)" ::: "memory");
  __syncthreads();
  STAMP();
  GAP();
  qk_first(w, make_iseq<32>{});
  asm volatile("s_nop 15" ::: "memory");
  GAP();
  {
    const int k0 = t0 * 64;
    float a = -INFINITY;
#pragma unroll
    for (int r = 0; r < 16; ++r) {
      float x0 = w.S[0][0][r], x1 = w.S[0][1][r];
      if (k0 + crow(r, w.hi) > q) x0 = -INFINITY;
      if (k0 + 32 + crow(r, w.hi) > q) x1 = -INFINITY;
      w.S[0][0][r] = x0; w.S[0][1][r] = x1;
      a = fmaxf(a, fmaxf(x0, x1));
    }
    auto rr = __builtin_amdgcn_permlane32_swap(__float_as_uint(a), __float_as_uint(a), false, false);
    w.pmax = fmaxf(__uint_as_float(rr[0]), __uint_as_float(rr[1]));
    w.m = fmaxf(w.m, w.pmax);
#pragma unroll
    for (int r = 0; r < 16; ++r) { w.S[0][0][r] -= w.m; w.S[0][1][r] -= w.m; w.negm[r] = -w.m; }
  }
  w.thr = q - (t0 + 1) * 64 - 4 * w.hi;
  __syncthreads();
  GAP();
  STAMP();
  const int jm = 2 * qb - t0;
  int jj = 0;
  for (; jj + 1 < n; jj += 2) {
    if (jj + 2 >= n) w.sk -= 32768;
    if (jj + 1 < jm) attn_step<0, true, false>(w); else attn_step<0, true, true>(w);
    if (jj + 2 < n) {
      if (jj + 3 >= n) w.sk -= 32768;
      if (jj + 2 < jm) attn_step<1, true, false>(w); else attn_step<1, true, true>(w);
    } else { attn_step<1, false, false>(w); jj = n; break; }
  }
  if (jj < n) attn_step<0, false, false>(w);
  asm volatile("s_nop 15" ::: "memory");
  STAMP();
}

__device__ __forceinline__ void ctx_put(u16* dst, int p, const f32x4& v, const f32x4& u) {
  unsigned a0 = pk2(v[0], v[1]), a1 = pk2(v[2], v[3]), b0 = pk2(u[0], u[1]), b1 = pk2(u[2], u[3]);
  auto r0 = __builtin_amdgcn_permlane32_swap(a0, b0, false, false);
  auto r1 = __builtin_amdgcn_permlane32_swap(a1, b1, false, false);
  u32x4 t = {r0[0], r1[0], r0[1], r1[1]};
  *reinterpret_cast<u32x4*>(dst + p * 512) = t;
}
template <int P> __device__ __forceinline__ void ctx_store1(u16* dst, float inv) {
  f32x4 v = o_read4<2 * P>(), u = o_read4<2 * P + 1>();
  v *= inv; u *= inv;
  ctx_put(dst, P, v, u);
}
template <int... Ps> __device__ __forceinline__ void ctx_store(u16* dst, float inv, iseq<Ps...>) { (ctx_store1<Ps>(dst, inv), ...); }
template <int I> __device__ __forceinline__ void part_store1(__amdgpu_buffer_rsrc_t rs, unsigned off) {
  const f32x4 v = o_read4<I>();
  __builtin_amdgcn_raw_buffer_store_b128(__builtin_bit_cast(u32x4, v), rs, off + I * 1024, 0, 16);
}
template <int... Is> __device__ __forceinline__ void part_store(__amdgpu_buffer_rsrc_t rs, unsigned off, iseq<Is...>) { (part_store1<Is>(rs, off), ...); }
template <int P> __device__ __forceinline__ void comb_store1(u16* dst, const f32x4* pu, float a1, float a2) {
  f32x4 v = o_read4<2 * P>(), u = o_read4<2 * P + 1>();
  v = v * a1 + pu[2 * P] * a2; u = u * a1 + pu[2 * P + 1] * a2;
  ctx_put(dst, P, v, u);
}
template <int... Ps> __device__ __forceinline__ void comb_store(u16* dst, __amdgpu_buffer_rsrc_t rs, unsigned off, float a1, float a2, iseq<Ps...>) {
  f32x4 pu[32];
#pragma unroll
  for (int i = 0; i < 32; ++i) pu[i] = __builtin_bit_cast(f32x4, __builtin_amdgcn_raw_buffer_load_b128(rs, off + i * 1024, 0, 16));
  (comb_store1<Ps>(dst, pu, a1, a2), ...);
}

__global__ __launch_bounds__(256, 1) void attn_kernel(char* __restrict__ ws) {
  extern __shared__ __attribute__((aligned(16))) char smem[];
  const u16* Qb = (const u16*)(ws + WS_Q);
  const u16* Kb = (const u16*)(ws + WS_K);
  const u16* Vt = (const u16*)(ws + WS_VT);
  u16* ctx = (u16*)(ws + WS_CTX);
  unsigned* cnt = (unsigned*)(ws + WS_CNT);
  const int xg = blockIdx.x & 7, r = blockIdx.x >> 3;
  const int h = xg * 2 + (r & 1), rr = r >> 1;
  const int i = rr >> 1, side = rr & 1;
  const u16* Kh = Kb + (size_t)h * S_ * 256;
  const u16* Vh = Vt + (size_t)h * S_ * 256;
  const int tid = threadIdx.x, wid = __builtin_amdgcn_readfirstlane(tid >> 6), lane = tid & 63, r32 = lane & 31, hi = lane >> 5;
  AW w;
  w.smem = smem; w.wid = wid; w.hi = hi; w.ninf = -INFINITY;
  const unsigned sb = (unsigned)(size_t)smem;
  w.voff = (unsigned)(wid * 8192 + lane * 16);
  w.m0b = sb + (unsigned)wid * 8192u;
  w.srd = __builtin_amdgcn_make_buffer_rsrc(ws + WS_K, 0, (int)(2 * S_ * HD_ * 2), 0x00020000);
#pragma unroll
  for (int s = 0; s < 8; ++s) w.ka[s] = sb + r32 * 512 + (((s * 2 + hi) ^ (r32 & 15)) << 4);
#pragma unroll
  for (int s = 0; s < 4; ++s) w.va[s] = sb + 65536 + r32 * 128 + (((s * 2 + hi) ^ ((r32 >> 1) & 7)) << 4);
  const int nseg = side ? 2 : 1;
  const int qb = 15 - i;
  const int tsplit = min(TSPLIT, 2 * (16 - i));
  const bool nosplit = tsplit == 2 * (16 - i);
  for (int seg = 0; seg < nseg; ++seg) {
    const bool full = (side == 1 && seg == 0);
    const int sqb = full ? i : qb;
    const int t0 = full ? 0 : (side ? tsplit : 0);
    const int t1 = full ? 2 * (i + 1) : (side ? 2 * (16 - i) : tsplit);
    if (t0 >= t1) return;
    attn_segment(w, Qb, Kh, Vh, h, sqb, t0, t1);
    if (full || nosplit) ctx_store(ctx + (size_t)((w.q >> 5) * 16 + h) * 8192 + lane * 8, 1.f / w.l, make_iseq<16>{});
  }
  if (nosplit) return;
  const int pair = h * 8 + i;
  __syncthreads();
  volatile unsigned* s_ticket = reinterpret_cast<volatile unsigned*>(smem);
  if (tid == 0) *s_ticket = atomicAdd(&cnt[pair], 1u);
  __syncthreads();
  const unsigned ticket = *s_ticket;
  const __amdgpu_buffer_rsrc_t prs = __builtin_amdgcn_make_buffer_rsrc(ws + WS_PART, 0, (int)(WS_END - WS_PART), 0x00020000);
  const unsigned poff = (unsigned)pair * 131072u + (unsigned)wid * 32768u + (unsigned)lane * 16u;
  const unsigned mloff = (unsigned)(WS_PML - WS_PART) + (unsigned)(pair * 256 + tid) * 8u;
  if (ticket == 0) {
    part_store(prs, poff, make_iseq<32>{});
    { u32x2 t = {__float_as_uint(w.m), __float_as_uint(w.l)}; __builtin_amdgcn_raw_buffer_store_b64(t, prs, mloff, 0, 16); }
    asm volatile("s_waitcnt vmcnt(0)" ::: "memory");
    __syncthreads();
    if (tid == 0) __hip_atomic_store(&cnt[128 + pair], 1u, __ATOMIC_RELAXED, __HIP_MEMORY_SCOPE_AGENT);
  } else {
    if (tid == 0) {
      while (__hip_atomic_load(&cnt[128 + pair], __ATOMIC_RELAXED, __HIP_MEMORY_SCOPE_AGENT) == 0u) __builtin_amdgcn_s_sleep(2);
    }
    __syncthreads();
    __builtin_amdgcn_fence(__ATOMIC_ACQUIRE, "wavefront");
    const u32x2 mlw = __builtin_amdgcn_raw_buffer_load_b64(prs, mloff, 0, 16);
    const float om = __uint_as_float(mlw[0]), ol = __uint_as_float(mlw[1]);
    const float mn = fmaxf(w.m, om);
    float a1 = __builtin_amdgcn_exp2f(w.m - mn), a2 = __builtin_amdgcn_exp2f(om - mn);
    const float inv = 1.f / (w.l * a1 + ol * a2);
    a1 *= inv; a2 *= inv;
    comb_store(ctx + (size_t)((w.q >> 5) * 16 + h) * 8192 + lane * 8, prs, poff, a1, a2, make_iseq<16>{});
  }
  STAMP();
}

__device__ __forceinline__ unsigned offB64(unsigned row, unsigned ch) {
  const unsigned f = ((row >> 1) & 1) | (((row >> 3) & 1) << 1);
  return 128u * row + 16u * (ch ^ (f << 1));
}
constexpr int ONS = 6;
struct OutCtx {
  const u16* ctx; const u16* act; const u16* wo; const u16* w2; char* smem; int m0, n0;
  const u16* pa[2]; const u16* pb; int astep; unsigned lb;
};
__device__ __forceinline__ void out_ptrs(OutCtx& c, int g) {
  const int tid = threadIdx.x;
#pragma unroll
  for (int i = 0; i < 2; ++i) {
    const int p = i * 512 + tid;
    const int row = p >> 3, chs = (p & 7) ^ ((row >> 1) & 7);
    const int gr = c.m0 + row;
    if (g < 64) c.pa[i] = c.ctx + (size_t)((c.m0 >> 5) + (p >> 8)) * 131072 + (size_t)g * 2048 + ((p >> 6) & 3) * 512 + (p & 63) * 8;
    else c.pa[i] = c.act + (size_t)gr * F_ + (g - 64) * 64 + chs * 8;
  }
  c.astep = g < 64 ? 2048 : 64;
  {
    const int p = tid;
    const int row = p >> 3; const unsigned f = ((row >> 1) & 1) | (((row >> 3) & 1) << 1);
    const int chs = (p & 7) ^ (f << 1);
    c.pb = (g < 64 ? c.wo + (size_t)(g * 64 + row) * M_ : c.w2 + (size_t)((g - 64) * 64 + row) * M_) + c.n0 + chs * 8;
  }
}
__device__ __forceinline__ void out_stage(OutCtx& c, int slot, bool adv) {
  const unsigned lb = c.lb + (unsigned)slot * 24576u;
  glds_asm(c.pa[0], lb); glds_asm(c.pa[1], lb + 8192); glds_asm(c.pb, lb + 16384);
  if (adv) { c.pa[0] += c.astep; c.pa[1] += c.astep; c.pb += 64 * M_; }
}
template <bool TILED>
__device__ __forceinline__ void out_compute(f32x4 (&acc)[2][2], const char* a, int wm, int wn, int lane) {
  const int fr = lane & 15, fq = lane >> 4;
  const char* b = a + 16384;
  bf16x8 af[2][2], bfr[2][2];
#pragma unroll
  for (int ks = 0; ks < 2; ++ks) {
#pragma unroll
    for (int mi = 0; mi < 2; ++mi) {
      const int row = wm * 32 + mi * 16 + fr, ch = ks * 4 + fq;
      if (TILED) af[ks][mi] = *reinterpret_cast<const bf16x8*>(a + ((wm * 4 + (ch >> 1)) * 64 + (ch & 1) * 32 + (row & 31)) * 16);
      else af[ks][mi] = *reinterpret_cast<const bf16x8*>(a + row * 128 + ((ch ^ ((row >> 1) & 7)) << 4));
    }
#pragma unroll
    for (int ni = 0; ni < 2; ++ni) {
      const int c = wn * 2 + ni, q = fr >> 2, pp = lane & 3;
      const unsigned a0 = offB64(32 * ks + 8 * fq + q, 2 * c + (pp >> 1)) + 8 * (pp & 1);
      const unsigned a1 = offB64(32 * ks + 8 * fq + 4 + q, 2 * c + (pp >> 1)) + 8 * (pp & 1);
      s16x4 lo = __builtin_amdgcn_ds_read_tr16_b64_v4i16((lds_s16x4*)(b + a0));
      s16x4 hi = __builtin_amdgcn_ds_read_tr16_b64_v4i16((lds_s16x4*)(b + a1));
      bfr[ks][ni] = bf16x8{lo[0], lo[1], lo[2], lo[3], hi[0], hi[1], hi[2], hi[3]};
    }
  }
#pragma unroll
  for (int ks = 0; ks < 2; ++ks)
#pragma unroll
    for (int mi = 0; mi < 2; ++mi)
#pragma unroll
      for (int ni = 0; ni < 2; ++ni)
        acc[mi][ni] = __builtin_amdgcn_mfma_f32_16x16x32_bf16(bfr[ks][ni], af[ks][mi], acc[mi][ni], 0, 0, 0);
}
template <bool TILED>
__device__ __forceinline__ void out_run(f32x4 (&acc)[2][2], OutCtx& c, int g0, int g1, int gend, int& slot, int wm, int wn, int lane) {
  for (int g = g0; g < g1; ++g) {
    asm volatile("s_waitcnt vmcnt(%c0)" :: "i"(3 * (ONS - 2)) : "memory");
    __builtin_amdgcn_s_barrier();
    if ((g & 7) == 0) STAMP3();
    __builtin_amdgcn_sched_barrier(0);
    const int gl = g + ONS - 1;
    if (gl == 64) out_ptrs(c, 64);
    int ls = slot + ONS - 1; if (ls >= ONS) ls -= ONS;
    out_stage(c, ls, gl + 1 < gend);
    out_compute<TILED>(acc, c.smem + slot * 24576, wm, wn, lane);
    asm volatile("s_waitcnt lgkmcnt(0)" ::: "memory");
    __builtin_amdgcn_sched_barrier(0);
    if (++slot == ONS) slot = 0;
  }
}

__global__ __launch_bounds__(512, 1) void out_kernel(const u16* __restrict__ x, const u16* __restrict__ wo, const u16* __restrict__ w2,
                                                     char* __restrict__ ws, u16* __restrict__ out) {
  extern __shared__ __attribute__((aligned(16))) char smem[];
  const int bx = blockIdx.x, half = (bx >> 3) & 1, tile = (bx >> 4) * 8 + (bx & 7);
  const int m0 = (tile & 15) * 128, n0 = (tile >> 4) * 64;
  const int tid = threadIdx.x, wid = tid >> 6, lane = tid & 63;
  const int wm = wid >> 1, wn = wid & 1, fr = lane & 15, fq = lane >> 4;
  OutCtx c;
  c.ctx = (const u16*)(ws + WS_CTX); c.act = (const u16*)(ws + WS_ACT); c.wo = wo; c.w2 = w2; c.smem = smem; c.m0 = m0; c.n0 = n0;
  c.lb = __builtin_amdgcn_readfirstlane((unsigned)(size_t)smem + (unsigned)wid * 1024u);
  f32x4 accs[2][2], accg[2][2];
#pragma unroll
  for (int i = 0; i < 2; ++i)
#pragma unroll
    for (int j = 0; j < 2; ++j) { accs[i][j] = f32x4{0.f, 0.f, 0.f, 0.f}; accg[i][j] = f32x4{0.f, 0.f, 0.f, 0.f}; }
  const int gs = half ? 46 : 0;
  STAMP3();
  out_ptrs(c, gs);
#pragma unroll
  for (int i = 0; i < ONS - 1; ++i) out_stage(c, i, true);
  int slot = 0;
  if (half == 0) {
    out_run<true>(accs, c, 0, 46, 46, slot, wm, wn, lane);
  } else {
    out_run<true>(accs, c, 46, 64, 92, slot, wm, wn, lane);
    out_run<false>(accg, c, 64, 92, 92, slot, wm, wn, lane);
  }
  asm volatile("s_waitcnt vmcnt(0)" ::: "memory");
  __syncthreads();
  STAMP3();
  unsigned* cnt = (unsigned*)(ws + WS_CNT);
  volatile unsigned* s_ticket = reinterpret_cast<volatile unsigned*>(smem);
  if (tid == 0) *s_ticket = atomicAdd(&cnt[256 + tile], 1u);
  __syncthreads();
  const unsigned ticket = *s_ticket;
  const __amdgpu_buffer_rsrc_t prs = __builtin_amdgcn_make_buffer_rsrc(ws + WS_OPART, 0, 112 * 65536, 0x00020000);
  const unsigned poff = (unsigned)tile * 65536u + (unsigned)tid * 16u;
  if (ticket == 0) {
#pragma unroll
    for (int i = 0; i < 4; ++i) __builtin_amdgcn_raw_buffer_store_b128(__builtin_bit_cast(u32x4, accs[i >> 1][i & 1]), prs, poff + i * 8192, 0, 16);
    if (half) {
#pragma unroll
      for (int i = 0; i < 4; ++i) __builtin_amdgcn_raw_buffer_store_b128(__builtin_bit_cast(u32x4, accg[i >> 1][i & 1]), prs, poff + 32768 + i * 8192, 0, 16);
    }
    asm volatile("s_waitcnt vmcnt(0)" ::: "memory");
    __syncthreads();
    if (tid == 0) __hip_atomic_store(&cnt[384 + tile], 1u, __ATOMIC_RELAXED, __HIP_MEMORY_SCOPE_AGENT);
    return;
  }
  if (tid == 0) {
    while (__hip_atomic_load(&cnt[384 + tile], __ATOMIC_RELAXED, __HIP_MEMORY_SCOPE_AGENT) == 0u) __builtin_amdgcn_s_sleep(2);
  }
  __syncthreads();
  __builtin_amdgcn_fence(__ATOMIC_ACQUIRE, "wavefront");
#pragma unroll
  for (int i = 0; i < 4; ++i) accs[i >> 1][i & 1] += __builtin_bit_cast(f32x4, __builtin_amdgcn_raw_buffer_load_b128(prs, poff + i * 8192, 0, 16));
  if (!half) {
#pragma unroll
    for (int i = 0; i < 4; ++i) accg[i >> 1][i & 1] = __builtin_bit_cast(f32x4, __builtin_amdgcn_raw_buffer_load_b128(prs, poff + 32768 + i * 8192, 0, 16));
  }
#pragma unroll
  for (int mi = 0; mi < 2; ++mi)
#pragma unroll
    for (int ni = 0; ni < 2; ++ni) {
      const int m = m0 + wm * 32 + mi * 16 + fr;
      const int n = n0 + wn * 32 + ni * 16 + fq * 4;
      const u32x2 xw = *reinterpret_cast<const u32x2*>(x + (size_t)m * M_ + n);
      float r[4];
#pragma unroll
      for (int j = 0; j < 4; ++j) {
        const float xv = (j & 1) ? __uint_as_float(xw[j >> 1] & 0xffff0000u) : __uint_as_float(xw[j >> 1] << 16);
        const float sb = rbf(accs[mi][ni][j]), gb = rbf(accg[mi][ni][j]);
        const float sg = rbf(gb / (1.f + __expf(-gb)));
        r[j] = xv + rbf(sb * sg);
      }
      u32x2 w = {pk2(r[0], r[1]), pk2(r[2], r[3])};
      *reinterpret_cast<u32x2*>(out + (size_t)m * M_ + n) = w;
    }
  STAMP3();
}

extern "C" void kernel_launch(void* const* d_in, const int* in_sizes, int n_in,
                              void* d_out, int out_size, void* d_ws, size_t ws_size,
                              hipStream_t stream) {
  const u16* x   = (const u16*)d_in[0];
  const u16* ckv = (const u16*)d_in[1];
  const u16* wq  = (const u16*)d_in[2];
  const u16* wuk = (const u16*)d_in[3];
  const u16* wuv = (const u16*)d_in[4];
  const u16* wo  = (const u16*)d_in[5];
  const u16* w1  = (const u16*)d_in[7];
  const u16* b1  = (const u16*)d_in[8];
  const u16* w2  = (const u16*)d_in[9];
  char* ws = (char*)d_ws;
  (void)hipFuncSetAttribute((const void*)attn_kernel, hipFuncAttributeMaxDynamicSharedMemorySize, 131072);
  (void)hipFuncSetAttribute((const void*)proj_kernel, hipFuncAttributeMaxDynamicSharedMemorySize, 131072);
  (void)hipFuncSetAttribute((const void*)out_kernel, hipFuncAttributeMaxDynamicSharedMemorySize, ONS * 24576);
  ln_kernel<<<S_ / 4, 256, 0, stream>>>(x, (u16*)(ws + WS_XN), (unsigned*)(ws + WS_CNT));
  proj_kernel<<<440, 512, 131072, stream>>>((const u16*)(ws + WS_XN), ckv, wq, wuk, wuv, w1, b1, ws);
  attn_kernel<<<256, 256, 131072, stream>>>(ws);
  out_kernel<<<224, 512, ONS * 24576, stream>>>(x, wo, w2, ws, (u16*)d_out);
}
```

```cpp
#include <hip/hip_runtime.h>
#include <hip/hip_bf16.h>
#include <stdint.h>
#include <utility>

typedef unsigned short u16;
typedef short bf16x8 __attribute__((ext_vector_type(8)));
typedef short s16x4 __attribute__((ext_vector_type(4)));
typedef float f32x4 __attribute__((ext_vector_type(4)));
typedef float f32x16 __attribute__((ext_vector_type(16)));
typedef unsigned u32x2 __attribute__((ext_vector_type(2)));
typedef unsigned u32x4 __attribute__((ext_vector_type(4)));
typedef __attribute__((address_space(3))) s16x4 lds_s16x4;

constexpr int S_ = 2048, M_ = 448, H_ = 16, D_ = 256, DC_ = 1024, F_ = 1792;
constexpr int HD_ = H_ * D_;

constexpr size_t WS_CNT  = 0;
constexpr size_t WS_XN   = 4096;
constexpr size_t WS_Q    = WS_XN + (size_t)S_ * 512 * 2;
constexpr size_t WS_K    = WS_Q + (size_t)S_ * HD_ * 2;
constexpr size_t WS_VT   = WS_K + (size_t)S_ * HD_ * 2;
constexpr size_t WS_ACT  = WS_VT + (size_t)S_ * HD_ * 2;
constexpr size_t WS_CTX  = WS_ACT + (size_t)S_ * F_ * 2;
constexpr size_t WS_PART = WS_CTX + (size_t)S_ * HD_ * 2;
constexpr size_t WS_PML  = WS_PART + (size_t)128 * 131072;
constexpr size_t WS_END  = WS_PML + (size_t)128 * 256 * 8;
constexpr size_t WS_OPART = WS_END;

__device__ __forceinline__ float bf2f(u16 v) { return __uint_as_float(((unsigned)v) << 16); }
__device__ __forceinline__ u16 f2bf(float f) {
  unsigned u = __float_as_uint(f);
  u += 0x7FFFu + ((u >> 16) & 1u);
  return (u16)(u >> 16);
}
__device__ __forceinline__ float rbf(float f) { return bf2f(f2bf(f)); }
typedef float f32x2 __attribute__((ext_vector_type(2)));
typedef __bf16 bf16x2_t __attribute__((ext_vector_type(2)));
__device__ __forceinline__ unsigned pk2(float lo, float hi) {
  f32x2 v = {lo, hi};
  bf16x2_t b = __builtin_convertvector(v, bf16x2_t);
  return *reinterpret_cast<unsigned*>(&b);
}
__host__ __device__ constexpr int crow(int r, int hi) { return (r & 3) + 8 * (r >> 2) + 4 * hi; }

#define GLDS16(gptr, ldsptr) \
  __builtin_amdgcn_global_load_lds((const __attribute__((address_space(1))) unsigned*)(gptr), \
                                   (__attribute__((address_space(3))) unsigned*)(ldsptr), 16, 0, 0)

__device__ __forceinline__ void glds_asm(const void* g, unsigned lds) {
  asm volatile("s_mov_b32 m0, %1\n\ts_nop 0\n\tglobal_load_lds_dwordx4 %0, off" :: "v"(g), "s"(lds) : "memory");
}

__global__ __launch_bounds__(256) void ln_kernel(const u16* __restrict__ x, u16* __restrict__ xn, unsigned* __restrict__ cnt) {
  const int wave = threadIdx.x >> 6, lane = threadIdx.x & 63;
  const int row = blockIdx.x * 4 + wave;
  if (blockIdx.x < 2) cnt[blockIdx.x * 256 + threadIdx.x] = 0;
  float v[8];
  const bool act = lane < 56;
  if (act) {
    u32x4 w = *reinterpret_cast<const u32x4*>(x + (size_t)row * M_ + lane * 8);
#pragma unroll
    for (int i = 0; i < 4; ++i) { v[2 * i] = __uint_as_float(w[i] << 16); v[2 * i + 1] = __uint_as_float(w[i] & 0xffff0000u); }
  } else {
#pragma unroll
    for (int i = 0; i < 8; ++i) v[i] = 0.f;
  }
  float s = 0.f;
#pragma unroll
  for (int i = 0; i < 8; ++i) s += v[i];
#pragma unroll
  for (int o = 32; o >= 1; o >>= 1) s += __shfl_xor(s, o);
  const float mean = s * (1.f / M_);
  float q = 0.f;
  if (act) {
#pragma unroll
    for (int i = 0; i < 8; ++i) { float d = v[i] - mean; q += d * d; }
  }
#pragma unroll
  for (int o = 32; o >= 1; o >>= 1) q += __shfl_xor(q, o);
  const float var = q * (1.f / M_);
  const float mb = rbf(mean), vb = rbf(var);
  const float den = rbf(sqrtf(rbf(vb + 1e-5f)));
  if (act) {
    u32x4 o;
#pragma unroll
    for (int i = 0; i < 4; ++i) {
      float a = rbf(rbf(v[2 * i] - mb) / den), b = rbf(rbf(v[2 * i + 1] - mb) / den);
      o[i] = (unsigned)f2bf(a) | ((unsigned)f2bf(b) << 16);
    }
    *reinterpret_cast<u32x4*>(xn + (size_t)row * 512 + lane * 8) = o;
  } else {
    *reinterpret_cast<u32x4*>(xn + (size_t)row * 512 + lane * 8) = u32x4{0u, 0u, 0u, 0u};
  }
}

__device__ __forceinline__ unsigned offB(unsigned row, unsigned ch) { return 256u * row + 16u * (ch ^ (((row & 3) << 2) | ((row >> 2) & 3))); }
__device__ __forceinline__ int ldsA_byte(int r, int c) {
  int st = (r >> 4) * 2 + (c >> 5), rr = r & 15, cc = c & 31, ob = rr * 64 + cc * 2;
  return st * 1024 + (ob ^ (((ob >> 9) & 1) << 5));
}
__device__ __forceinline__ void stageA_rc(int b, int& R, int& C) {
  int st = b / 1024, sb = b % 1024, swz = sb ^ (((sb >> 9) & 1) << 5);
  R = (st >> 1) * 16 + swz / 64; C = (st & 1) * 32 + (swz % 64) / 2;
}

template <int MODE>
__device__ __forceinline__ void gemm256(const u16* __restrict__ A, int lda, const u16* __restrict__ B, int ldb, int nt, int kmax,
                                        int brow, int bcol, char* shm, u16* __restrict__ out, const u16* __restrict__ bias) {
  constexpr int HALF = 128, HTB = 16384;
#define SA(b, h) (shm + ((b) * 2 + (h)) * HTB)
#define SB(b, h) (shm + (4 + (b) * 2 + (h)) * HTB)
#define BLDS(voff, srd, soff, ldsaddr) asm volatile("s_mov_b32 m0, %3\n\ts_nop 0\n\tbuffer_load_dwordx4 %0, %1, %2 offen lds" \
    :: "v"(voff), "s"(srd), "s"(soff), "s"(ldsaddr) : "memory")
#define STAGE_A(T, P, br, kt) do { const unsigned _so = (unsigned)(((br) * lda + (kt) * 64) * 2); \
    BLDS(voA0, srdA, _so, ldsw + (P)); BLDS(voA1, srdA, _so, ldsw + (P) + 8192u); } while (0)
#define STAGE_B(T, P, bc, kt) do { int _kt = (kt); if (MODE >= 2) _kt = min(_kt, kmax); \
    const unsigned _so = (unsigned)((_kt * 64 * ldb + (bc)) * 2); \
    BLDS(voB0, srdB, _so, ldsw + (P)); BLDS(voB1, srdB, _so, ldsw + (P) + 8192u); } while (0)
#define OA(b, h) ((unsigned)(((b) * 2 + (h)) * HTB))
#define OB(b, h) ((unsigned)((4 + (b) * 2 + (h)) * HTB))
#define LDA(dst, b, h) for (int m = 0; m < 4; ++m) for (int k = 0; k < 2; ++k) \
    dst[m][k] = *reinterpret_cast<const bf16x8*>(SA(b, h) + ldsA_byte(wr * 64 + m * 16 + fr, k * 32 + fq * 8))
#define LDB(dst, b, h) for (int n = 0; n < 2; ++n) for (int k = 0; k < 2; ++k) { \
    const unsigned _a0 = offB(32 * k + 8 * fq + (fr >> 2), 2 * (wc * 2 + n) + ((lane & 3) >> 1)) + 8 * (lane & 1); \
    const unsigned _a1 = offB(32 * k + 8 * fq + 4 + (fr >> 2), 2 * (wc * 2 + n) + ((lane & 3) >> 1)) + 8 * (lane & 1); \
    s16x4 _lo = __builtin_amdgcn_ds_read_tr16_b64_v4i16((lds_s16x4*)(SB(b, h) + _a0)); \
    s16x4 _hi = __builtin_amdgcn_ds_read_tr16_b64_v4i16((lds_s16x4*)(SB(b, h) + _a1)); \
    dst[n][k] = bf16x8{_lo[0], _lo[1], _lo[2], _lo[3], _hi[0], _hi[1], _hi[2], _hi[3]}; }
#define MMA(ai, bj, At_, Bt_) do { __builtin_amdgcn_s_setprio(1); \
    for (int m = 0; m < 4; ++m) for (int n = 0; n < 2; ++n) for (int k = 0; k < 2; ++k) { \
      if (MODE == 1) acc[ai][bj][m][n] = __builtin_amdgcn_mfma_f32_16x16x32_bf16(At_[m][k], Bt_[n][k], acc[ai][bj][m][n], 0, 0, 0); \
      else           acc[ai][bj][m][n] = __builtin_amdgcn_mfma_f32_16x16x32_bf16(Bt_[n][k], At_[m][k], acc[ai][bj][m][n], 0, 0, 0); } \
    __builtin_amdgcn_s_setprio(0); } while (0)
#define WAIT_V(n) asm volatile("s_waitcnt vmcnt(" #n ")" ::: "memory")
#define WAIT_L(n) asm volatile("s_waitcnt lgkmcnt(" #n ")" ::: "memory")
#define BAR __builtin_amdgcn_s_barrier()
#define SCHED __builtin_amdgcn_sched_barrier(0)
  int wid = threadIdx.x >> 6, lane = threadIdx.x & 63, wr = wid >> 2, wc = wid & 3, fr = lane & 15, fq = lane >> 4;
  f32x4 acc[2][2][4][2] = {};
  bf16x8 At[4][2], B0[2][2], B1[2][2];
  const int tx = threadIdx.x;
  unsigned voA0, voA1, voB0, voB1;
  { int r, c; stageA_rc(tx * 16, r, c); voA0 = (unsigned)((r * lda + c) * 2); stageA_rc(tx * 16 + 8192, r, c); voA1 = (unsigned)((r * lda + c) * 2);
    const int r0 = tx >> 4, r1 = r0 + 32;
    voB0 = (unsigned)((r0 * ldb + ((tx & 15) ^ (((r0 & 3) << 2) | ((r0 >> 2) & 3))) * 8) * 2);
    voB1 = (unsigned)((r1 * ldb + ((tx & 15) ^ (((r1 & 3) << 2) | ((r1 >> 2) & 3))) * 8) * 2); }
  const __amdgpu_buffer_rsrc_t srdA = __builtin_amdgcn_make_buffer_rsrc((void*)A, 0, 0x7fffffff, 0x00020000);
  const __amdgpu_buffer_rsrc_t srdB = __builtin_amdgcn_make_buffer_rsrc((void*)B, 0, 0x7fffffff, 0x00020000);
  const unsigned ldsw = __builtin_amdgcn_readfirstlane((unsigned)(size_t)shm + (unsigned)(tx >> 6) * 1024u);
  STAGE_B(tx, OB(0, 0), bcol, 0); STAGE_A(tx, OA(0, 0), brow, 0);
  STAGE_B(tx, OB(0, 1), bcol + HALF, 0); STAGE_A(tx, OA(0, 1), brow + HALF, 0);
  if (wr == 1) BAR;
  WAIT_V(4); BAR;
  STAGE_B(tx, OB(1, 0), bcol, 1); STAGE_A(tx, OA(1, 0), brow, 1); STAGE_B(tx, OB(1, 1), bcol + HALF, 1);
  WAIT_V(6); BAR;
  for (int t = 0; t < nt - 2; t += 2) {
    LDB(B0, 0, 0); SCHED; LDA(At, 0, 0); STAGE_A(tx, OA(1, 1), brow + HALF, t + 1);
    WAIT_L(8); BAR; WAIT_L(0); MMA(0, 0, At, B0); BAR; SCHED;
    LDB(B1, 0, 1); STAGE_B(tx, OB(0, 0), bcol, t + 2);
    BAR; WAIT_L(0); MMA(0, 1, At, B1); BAR;
    LDA(At, 0, 1); STAGE_A(tx, OA(0, 0), brow, t + 2);
    BAR; WAIT_L(0); MMA(1, 0, At, B0); BAR; SCHED;
    STAGE_B(tx, OB(0, 1), bcol + HALF, t + 2);
    WAIT_V(6); BAR; MMA(1, 1, At, B1); BAR;
    LDB(B0, 1, 0); SCHED; LDA(At, 1, 0); STAGE_A(tx, OA(0, 1), brow + HALF, t + 2);
    WAIT_L(8); BAR; WAIT_L(0); MMA(0, 0, At, B0); BAR; SCHED;
    LDB(B1, 1, 1); STAGE_B(tx, OB(1, 0), bcol, t + 3);
    BAR; WAIT_L(0); MMA(0, 1, At, B1); BAR;
    LDA(At, 1, 1); STAGE_A(tx, OA(1, 0), brow, t + 3);
    BAR; WAIT_L(0); MMA(1, 0, At, B0); BAR; SCHED;
    STAGE_B(tx, OB(1, 1), bcol + HALF, t + 3);
    WAIT_V(6); BAR; MMA(1, 1, At, B1); BAR;
  }
  int tz = threadIdx.x; asm volatile("" : "+v"(tz)); wid = tz >> 6; lane = tz & 63; wr = wid >> 2; wc = wid & 3; fr = lane & 15; fq = lane >> 4;
  { LDB(B0, 0, 0); WAIT_V(0); LDA(At, 0, 0); STAGE_A(tz, OA(1, 1), brow + HALF, nt - 1);
    BAR; WAIT_L(0); MMA(0, 0, At, B0); BAR;
    LDB(B1, 0, 1); BAR; WAIT_L(0); MMA(0, 1, At, B1); BAR;
    LDA(At, 0, 1); WAIT_V(4); BAR; WAIT_L(0); MMA(1, 0, At, B0); MMA(1, 1, At, B1); BAR; }
  if (MODE < 2) {
    LDB(B0, 1, 0); LDA(At, 1, 0); WAIT_V(2); BAR; WAIT_L(0); MMA(0, 0, At, B0); BAR;
    LDB(B1, 1, 1); WAIT_V(0); BAR; WAIT_L(0); MMA(0, 1, At, B1); BAR;
    LDA(At, 1, 1); BAR; WAIT_L(0); MMA(1, 0, At, B0); MMA(1, 1, At, B1); BAR;
  } else { WAIT_V(0); }
  if (wr == 0) BAR;
#pragma unroll
  for (int ai = 0; ai < 2; ++ai)
#pragma unroll
  for (int bj = 0; bj < 2; ++bj)
#pragma unroll
  for (int mi = 0; mi < 4; ++mi)
#pragma unroll
  for (int ni = 0; ni < 2; ++ni) {
      const f32x4 v = acc[ai][bj][mi][ni];
      const int mb = brow + ai * HALF + wr * 64 + mi * 16, nb = bcol + bj * HALF + wc * 32 + ni * 16;
      if (MODE == 1) {
        const int n = nb + fr, m = mb + fq * 4;
        const int h = n >> 8, d = n & 255, tile = m >> 6, kk = m & 63;
        const int ks = kk >> 4, qq = (kk >> 2) & 3;
        const int ch = ks * 2 + (qq & 1), sub = qq >> 1;
        const size_t off = (((size_t)(h * 32 + tile) * 256 + d) * 64) + ((ch ^ ((d >> 1) & 7)) << 3) + sub * 4;
        u32x2 w = {pk2(v[0], v[1]), pk2(v[2], v[3])};
        *reinterpret_cast<u32x2*>(out + off) = w;
      } else {
        const int m = mb + fr, n = nb + fq * 4;
        if (MODE == 0) {
          const int h = n >> 8, d = n & 255, ch = d >> 3;
          const size_t off = ((size_t)h * S_ + m) * 256 + ((ch ^ (m & 15)) << 3) + (d & 7);
          u32x2 w = {pk2(v[0], v[1]), pk2(v[2], v[3])};
          *reinterpret_cast<u32x2*>(out + off) = w;
        } else if (MODE == 2) {
          constexpr float QS = 0.0625f * 1.4426950408889634f;
          u32x2 w = {pk2(v[0] * QS, v[1] * QS), pk2(v[2] * QS, v[3] * QS)};
          const int h = n >> 8, d = n & 255;
          const size_t off = ((size_t)((m >> 5) * 16 + h) * 16 + (d >> 4)) * 512 + (((d >> 3) & 1) * 32 + (m & 31)) * 8 + (d & 7);
          *reinterpret_cast<u32x2*>(out + off) = w;
        } else {
          float r[4];
          const u32x2 bw = *reinterpret_cast<const u32x2*>(bias + n);
#pragma unroll
          for (int j = 0; j < 4; ++j) {
            const float bv = (j & 1) ? __uint_as_float(bw[j >> 1] & 0xffff0000u) : __uint_as_float(bw[j >> 1] << 16);
            const float hh = rbf(rbf(v[j]) + bv);
            const float e = __expf(fminf(hh, 30.f));
            const float nn = e * (e + 2.f);
            r[j] = hh * nn * __builtin_amdgcn_rcpf(nn + 2.f);
          }
          u32x2 w = {pk2(r[0], r[1]), pk2(r[2], r[3])};
          *reinterpret_cast<u32x2*>(out + (size_t)m * F_ + n) = w;
        }
      }
    }
#undef SA
#undef SB
#undef STAGE_A
#undef STAGE_B
#undef OA
#undef OB
#undef BLDS
#undef LDA
#undef LDB
#undef MMA
}

__global__ __launch_bounds__(512, 2) void proj_kernel(const u16* __restrict__ xn, const u16* __restrict__ ckv,
                                                      const u16* __restrict__ wq, const u16* __restrict__ wuk,
                                                      const u16* __restrict__ wuv, const u16* __restrict__ w1,
                                                      const u16* __restrict__ b1, char* __restrict__ ws) {
  extern __shared__ __attribute__((aligned(16))) char smem[];
  int b = blockIdx.x;
  if (b < 256) {
    const int x = b & 7, i = b >> 3;
    const int ct = x * 4 + (i >> 3), rt = i & 7;
    if (ct < 16) gemm256<0>(ckv, DC_, wuk, HD_, DC_ / 64, DC_ - 1, rt * 256, ct * 256, smem, (u16*)(ws + WS_K), nullptr);
    else         gemm256<1>(ckv, DC_, wuv, HD_, DC_ / 64, DC_ - 1, rt * 256, (ct - 16) * 256, smem, (u16*)(ws + WS_VT), nullptr);
  } else if (b < 384) {
    b -= 256;
    const int x = b & 7, i = b >> 3;
    const int ct = x * 2 + (i >> 3), rt = i & 7;
    gemm256<2>(xn, 512, wq, HD_, 8, 6, rt * 256, ct * 256, smem, (u16*)(ws + WS_Q), nullptr);
  } else {
    b -= 384;
    gemm256<3>(xn, 512, w1, F_, 8, 6, (b & 7) * 256, (b >> 3) * 256, smem, (u16*)(ws + WS_ACT), b1);
  }
}

constexpr float SC2 = 0.0625f * 1.4426950408889634f;
constexpr float THR_LOG2 = 8.f;
#define ACLOB "a0","a1","a2","a3","a4","a5","a6","a7","a8","a9","a10","a11","a12","a13","a14","a15","a16","a17","a18","a19","a20","a21","a22","a23","a24","a25","a26","a27","a28","a29","a30","a31","a32","a33","a34","a35","a36","a37","a38","a39","a40","a41","a42","a43","a44","a45","a46","a47","a48","a49","a50","a51","a52","a53","a54","a55","a56","a57","a58","a59","a60","a61","a62","a63","a64","a65","a66","a67","a68","a69","a70","a71","a72","a73","a74","a75","a76","a77","a78","a79","a80","a81","a82","a83","a84","a85","a86","a87","a88","a89","a90","a91","a92","a93","a94","a95","a96","a97","a98","a99","a100","a101","a102","a103","a104","a105","a106","a107","a108","a109","a110","a111","a112","a113","a114","a115","a116","a117","a118","a119","a120","a121","a122","a123","a124","a125","a126","a127","a128","a129","a130","a131","a132","a133","a134","a135","a136","a137","a138","a139","a140","a141","a142","a143","a144","a145","a146","a147","a148","a149","a150","a151","a152","a153","a154","a155","a156","a157","a158","a159","a160","a161","a162","a163","a164","a165","a166","a167","a168","a169","a170","a171","a172","a173","a174","a175","a176","a177","a178","a179","a180","a181","a182","a183","a184","a185","a186","a187","a188","a189","a190","a191"
#define GAP() __builtin_amdgcn_sched_barrier(0)
template <int... Is> using iseq = std::integer_sequence<int, Is...>;
template <int N> using make_iseq = std::make_integer_sequence<int, N>;

#define TSPLIT 18
#define PD 8
struct AW {
  f32x16 S[2][2];
  u32x4 P[4];
  bf16x8 kf[32];
  bf16x8 vf[32];
  float pe[32];
  f32x16 negm;
  float m, l, rs0, rs1, pm0, pm1, pmax, ninf;
  unsigned ka[8];
  unsigned va[4];
  unsigned voff;
  unsigned sk, sv;
  unsigned m0b;
  __amdgpu_buffer_rsrc_t srd;
  char* smem; int wid;
  int thr, q, hi;
};

template <int OFF> __device__ __forceinline__ void ds_ld(bf16x8& dst, unsigned addr) {
  asm volatile("ds_read_b128 %0, %1 offset:%c2" : "=v"(dst) : "v"(addr), "i"(OFF) : "memory");
}
template <int X, int M> constexpr int k_off() { return ((M >> 1) >> 3) * 256 + (M & 1) * 16384 + X * 32768; }
constexpr int pv_d(int M) { return M & 7; }
constexpr int pv_ks(int M) { return M >> 3; }
template <int X, int M> constexpr int v_off() { return pv_d(M) * 4096 + X * 32768; }
template <int X, int M> __device__ __forceinline__ void k_load(AW& w) { ds_ld<k_off<X, M>()>(w.kf[M], w.ka[(M >> 1) & 7]); }
template <int X, int M> __device__ __forceinline__ void v_load(AW& w) { ds_ld<v_off<X, M>()>(w.vf[M], w.va[pv_ks(M)]); }
template <int X, int... Ms> __device__ __forceinline__ void k_pre(AW& w, iseq<Ms...>) { (k_load<X, Ms>(w), ...); }
template <int X, int M, int W, bool FIRST = false> __device__ __forceinline__ void qk_mfma(AW& w) {
  constexpr int st = M >> 1, half = M & 1;
  if constexpr (W >= 0) asm volatile("s_waitcnt lgkmcnt(%c0)" :: "i"(W) : "memory");
  if constexpr (st == 0 && FIRST)
    asm volatile("v_mfma_f32_32x32x16_bf16 %0, %1, a[%c2:%c3], 0" : "=&v"(w.S[X][half]) : "v"(w.kf[M]), "i"(128 + 4 * st), "i"(128 + 4 * st + 3) : ACLOB);
  else if constexpr (st == 0)
    asm volatile("v_mfma_f32_32x32x16_bf16 %0, %1, a[%c2:%c3], %4" : "=&v"(w.S[X][half]) : "v"(w.kf[M]), "i"(128 + 4 * st), "i"(128 + 4 * st + 3), "v"(w.negm) : ACLOB);
  else
    asm volatile("v_mfma_f32_32x32x16_bf16 %0, %1, a[%c2:%c3], %0" : "+v"(w.S[X][half]) : "v"(w.kf[M]), "i"(128 + 4 * st), "i"(128 + 4 * st + 3) : ACLOB);
}
template <int M, int W> __device__ __forceinline__ void pv_mfma(AW& w) {
  constexpr int d = pv_d(M), ks = pv_ks(M);
  if constexpr (W >= 0) asm volatile("s_waitcnt lgkmcnt(%c0)" :: "i"(W) : "memory");
  asm volatile("v_mfma_f32_32x32x16_bf16 a[%c2:%c3], %0, %1, a[%c2:%c3]" :: "v"(w.vf[M]), "v"(w.P[ks]), "i"(16 * d), "i"(16 * d + 15) : ACLOB);
}
template <int M> constexpr int wcnt() { return (M & 1) ? -1 : ((M + PD <= 32) ? PD - 2 : (32 - M - 2 > 0 ? 32 - M - 2 : 0)); }
template <int XC, int M> __device__ __forceinline__ void sm_gap(AW& w) {
  const float sv = w.S[XC][M >> 4][M & 15];
  if constexpr (M == 0) {
    asm volatile("v_exp_f32 %0, %1" : "=&v"(w.pe[0]) : "v"(sv));
  } else if constexpr ((M - 1) & 1) {
    constexpr int E = M - 1, ks = E >> 3, dw = (E >> 1) & 3;
    unsigned pk;
    asm volatile("v_exp_f32 %0, %3\n\tv_add_f32 %1, %1, %5\n\tv_cvt_pk_bf16_f32 %2, %4, %5"
                 : "=&v"(w.pe[M]), "+v"(w.rs1), "=&v"(pk) : "v"(sv), "v"(w.pe[E - 1]), "v"(w.pe[E]));
    w.P[ks][dw] = pk;
  } else {
    asm volatile("v_exp_f32 %0, %2\n\tv_add_f32 %1, %1, %3"
                 : "=&v"(w.pe[M]), "+v"(w.rs0) : "v"(sv), "v"(w.pe[M - 1]));
  }
}
template <int XC, bool DOQK, int M> __device__ __forceinline__ void gapA(AW& w) {
  constexpr int XN = XC ^ 1;
  if constexpr (DOQK) {
    qk_mfma<XN, M, (M & 1) ? -1 : PD - 2>(w);
    if constexpr (M + PD < 32) k_load<XN, M + PD>(w);
  }
  if constexpr (M >= 32 - PD) v_load<XC, M - (32 - PD)>(w);
  if constexpr (DOQK && (M & 1) == 1) {
    constexpr int pc = M >> 1, g = (pc >> 2) & 1;
    constexpr unsigned ldsb = (pc < 8 ? XC * 32768u : 65536u + XN * 32768u) + g * 4096u;
    const unsigned soff = (pc < 8 ? w.sk : w.sv) + g * 4096u;
    if constexpr ((pc & 3) == 0)
      asm volatile("s_mov_b32 m0, %0\n\ts_nop 0\n\tbuffer_load_dwordx4 %1, %2, %3 offen lds"
                   :: "s"(w.m0b + ldsb), "v"(w.voff), "s"(w.srd), "s"(soff) : "memory");
    else
      asm volatile("buffer_load_dwordx4 %0, %1, %2 offen offset:%c3 lds"
                   :: "v"(w.voff), "s"(w.srd), "s"(soff), "i"((pc & 3) * 1024) : "memory");
  }
  sm_gap<XC, M>(w);
  GAP();
}
template <int XC, bool DOQK, bool MASK, int M> __device__ __forceinline__ void gapB(AW& w) {
  constexpr int XN = XC ^ 1;
  pv_mfma<M, wcnt<M>()>(w);
  if constexpr (M + PD < 32) v_load<XC, M + PD>(w);
  if constexpr (DOQK) {
    if constexpr (M < 0) {
      constexpr int g = (M >> 2) & 1;
      constexpr unsigned ldsb = (M < 8 ? 65536u + XN * 32768u : XC * 32768u) + g * 4096u;
      const unsigned soff = (M < 8 ? w.sv : w.sk) + g * 4096u;
      if constexpr ((M & 3) == 0)
        asm volatile("s_mov_b32 m0, %0\n\ts_nop 0\n\tbuffer_load_dwordx4 %1, %2, %3 offen lds"
                     :: "s"(w.m0b + ldsb), "v"(w.voff), "s"(w.srd), "s"(soff) : "memory");
      else
        asm volatile("buffer_load_dwordx4 %0, %1, %2 offen offset:%c3 lds"
                     :: "v"(w.voff), "s"(w.srd), "s"(soff), "i"((M & 3) * 1024) : "memory");
    }
    if constexpr (M >= 2 && M < 18) {
      constexpr int r = M - 2;
      float a = w.S[XN][0][r], b = w.S[XN][1][r];
      float& pm = (r & 1) ? w.pm1 : w.pm0;
      if constexpr (MASK) {
        asm volatile("v_cmp_gt_i32 vcc, %c5, %3\n\tv_cndmask_b32 %0, %0, %4, vcc\n\tv_cmp_gt_i32 vcc, %c6, %3\n\tv_cndmask_b32 %1, %1, %4, vcc\n\tv_max3_f32 %2, %2, %0, %1"
                     : "+v"(a), "+v"(b), "+v"(pm) : "v"(w.thr), "v"(w.ninf), "i"(crow(r, 0)), "i"(crow(r, 0) + 32) : "vcc");
        w.S[XN][0][r] = a; w.S[XN][1][r] = b;
      } else {
        asm volatile("v_max3_f32 %0, %0, %1, %2" : "+v"(pm) : "v"(a), "v"(b));
      }
    }
  }
  GAP();
}
template <int XN> __device__ __forceinline__ void attn_decide(AW& w) {
  if (__builtin_expect(!__all(w.pmax <= THR_LOG2), 0)) {
    const float dl = fmaxf(w.pmax, 0.f);
    const float alpha = __builtin_amdgcn_exp2f(-dl);
    w.m += dl; w.l *= alpha;
#pragma unroll
    for (int r = 0; r < 16; ++r) { w.S[XN][0][r] -= dl; w.S[XN][1][r] -= dl; w.negm[r] = -w.m; }
    float t0, t1;
    asm volatile("s_nop 7\n\ts_nop 7\n\t"
    "v_accvgpr_read_b32 %0, a0\n\tv_mul_f32 %0, %2, %0\n\tv_accvgpr_write_b32 a0, %0\n\t"
    "v_accvgpr_read_b32 %1, a1\n\tv_mul_f32 %1, %2, %1\n\tv_accvgpr_write_b32 a1, %1\n\t"
    "v_accvgpr_read_b32 %0, a2\n\tv_mul_f32 %0, %2, %0\n\tv_accvgpr_write_b32 a2, %0\n\t"
    "v_accvgpr_read_b32 %1, a3\n\tv_mul_f32 %1, %2, %1\n\tv_accvgpr_write_b32 a3, %1\n\t"
    "v_accvgpr_read_b32 %0, a4\n\tv_mul_f32 %0, %2, %0\n\tv_accvgpr_write_b32 a4, %0\n\t"
    "v_accvgpr_read_b32 %1, a5\n\tv_mul_f32 %1, %2, %1\n\tv_accvgpr_write_b32 a5, %1\n\t"
    "v_accvgpr_read_b32 %0, a6\n\tv_mul_f32 %0, %2, %0\n\tv_accvgpr_write_b32 a6, %0\n\t"
    "v_accvgpr_read_b32 %1, a7\n\tv_mul_f32 %1, %2, %1\n\tv_accvgpr_write_b32 a7, %1\n\t"
    "v_accvgpr_read_b32 %0, a8\n\tv_mul_f32 %0, %2, %0\n\tv_accvgpr_write_b32 a8, %0\n\t"
    "v_accvgpr_read_b32 %1, a9\n\tv_mul_f32 %1, %2, %1\n\tv_accvgpr_write_b32 a9, %1\n\t"
    "v_accvgpr_read_b32 %0, a10\n\tv_mul_f32 %0, %2, %0\n\tv_accvgpr_write_b32 a10, %0\n\t"
    "v_accvgpr_read_b32 %1, a11\n\tv_mul_f32 %1, %2, %1\n\tv_accvgpr_write_b32 a11, %1\n\t"
    "v_accvgpr_read_b32 %0, a12\n\tv_mul_f32 %0, %2, %0\n\tv_accvgpr_write_b32 a12, %0\n\t"
    "v_accvgpr_read_b32 %1, a13\n\tv_mul_f32 %1, %2, %1\n\tv_accvgpr_write_b32 a13, %1\n\t"
    "v_accvgpr_read_b32 %0, a14\n\tv_mul_f32 %0, %2, %0\n\tv_accvgpr_write_b32 a14, %0\n\t"
    "v_accvgpr_read_b32 %1, a15\n\tv_mul_f32 %1, %2, %1\n\tv_accvgpr_write_b32 a15, %1\n\t"
    "v_accvgpr_read_b32 %0, a16\n\tv_mul_f32 %0, %2, %0\n\tv_accvgpr_write_b32 a16, %0\n\t"
    "v_accvgpr_read_b32 %1, a17\n\tv_mul_f32 %1, %2, %1\n\tv_accvgpr_write_b32 a17, %1\n\t"
    "v_accvgpr_read_b32 %0, a18\n\tv_mul_f32 %0, %2, %0\n\tv_accvgpr_write_b32 a18, %0\n\t"
    "v_accvgpr_read_b32 %1, a19\n\tv_mul_f32 %1, %2, %1\n\tv_accvgpr_write_b32 a19, %1\n\t"
    "v_accvgpr_read_b32 %0, a20\n\tv_mul_f32 %0, %2, %0\n\tv_accvgpr_write_b32 a20, %0\n\t"
    "v_accvgpr_read_b32 %1, a21\n\tv_mul_f32 %1, %2, %1\n\tv_accvgpr_write_b32 a21, %1\n\t"
    "v_accvgpr_read_b32 %0, a22\n\tv_mul_f32 %0, %2, %0\n\tv_accvgpr_write_b32 a22, %0\n\t"
    "v_accvgpr_read_b32 %1, a23\n\tv_mul_f32 %1, %2, %1\n\tv_accvgpr_write_b32 a23, %1\n\t"
    "v_accvgpr_read_b32 %0, a24\n\tv_mul_f32 %0, %2, %0\n\tv_accvgpr_write_b32 a24, %0\n\t"
    "v_accvgpr_read_b32 %1, a25\n\tv_mul_f32 %1, %2, %1\n\tv_accvgpr_write_b32 a25, %1\n\t"
    "v_accvgpr_read_b32 %0, a26\n\tv_mul_f32 %0, %2, %0\n\tv_accvgpr_write_b32 a26, %0\n\t"
    "v_accvgpr_read_b32 %1, a27\n\tv_mul_f32 %1, %2, %1\n\tv_accvgpr_write_b32 a27, %1\n\t"
    "v_accvgpr_read_b32 %0, a28\n\tv_mul_f32 %0, %2, %0\n\tv_accvgpr_write_b32 a28, %0\n\t"
    "v_accvgpr_read_b32 %1, a29\n\tv_mul_f32 %1, %2, %1\n\tv_accvgpr_write_b32 a29, %1\n\t"
    "v_accvgpr_read_b32 %0, a30\n\tv_mul_f32 %0, %2, %0\n\tv_accvgpr_write_b32 a30, %0\n\t"
    "v_accvgpr_read_b32 %1, a31\n\tv_mul_f32 %1, %2, %1\n\tv_accvgpr_write_b32 a31, %1\n\t"
    "v_accvgpr_read_b32 %0, a32\n\tv_mul_f32 %0, %2, %0\n\tv_accvgpr_write_b32 a32, %0\n\t"
    "v_accvgpr_read_b32 %1, a33\n\tv_mul_f32 %1, %2, %1\n\tv_accvgpr_write_b32 a33, %1\n\t"
    "v_accvgpr_read_b32 %0, a34\n\tv_mul_f32 %0, %2, %0\n\tv_accvgpr_write_b32 a34, %0\n\t"
    "v_accvgpr_read_b32 %1, a35\n\tv_mul_f32 %1, %2, %1\n\tv_accvgpr_write_b32 a35, %1\n\t"
    "v_accvgpr_read_b32 %0, a36\n\tv_mul_f32 %0, %2, %0\n\tv_accvgpr_write_b32 a36, %0\n\t"
    "v_accvgpr_read_b32 %1, a37\n\tv_mul_f32 %1, %2, %1\n\tv_accvgpr_write_b32 a37, %1\n\t"
    "v_accvgpr_read_b32 %0, a38\n\tv_mul_f32 %0, %2, %0\n\tv_accvgpr_write_b32 a38, %0\n\t"
    "v_accvgpr_read_b32 %1, a39\n\tv_mul_f32 %1, %2, %1\n\tv_accvgpr_write_b32 a39, %1\n\t"
    "v_accvgpr_read_b32 %0, a40\n\tv_mul_f32 %0, %2, %0\n\tv_accvgpr_write_b32 a40, %0\n\t"
    "v_accvgpr_read_b32 %1, a41\n\tv_mul_f32 %1, %2, %1\n\tv_accvgpr_write_b32 a41, %1\n\t"
    "v_accvgpr_read_b32 %0, a42\n\tv_mul_f32 %0, %2, %0\n\tv_accvgpr_write_b32 a42, %0\n\t"
    "v_accvgpr_read_b32 %1, a43\n\tv_mul_f32 %1, %2, %1\n\tv_accvgpr_write_b32 a43, %1\n\t"
    "v_accvgpr_read_b32 %0, a44\n\tv_mul_f32 %0, %2, %0\n\tv_accvgpr_write_b32 a44, %0\n\t"
    "v_accvgpr_read_b32 %1, a45\n\tv_mul_f32 %1, %2, %1\n\tv_accvgpr_write_b32 a45, %1\n\t"
    "v_accvgpr_read_b32 %0, a46\n\tv_mul_f32 %0, %2, %0\n\tv_accvgpr_write_b32 a46, %0\n\t"
    "v_accvgpr_read_b32 %1, a47\n\tv_mul_f32 %1, %2, %1\n\tv_accvgpr_write_b32 a47, %1\n\t"
    "v_accvgpr_read_b32 %0, a48\n\tv_mul_f32 %0, %2, %0\n\tv_accvgpr_write_b32 a48, %0\n\t"
    "v_accvgpr_read_b32 %1, a49\n\tv_mul_f32 %1, %2, %1\n\tv_accvgpr_write_b32 a49, %1\n\t"
    "v_accvgpr_read_b32 %0, a50\n\tv_mul_f32 %0, %2, %0\n\tv_accvgpr_write_b32 a50, %0\n\t"
    "v_accvgpr_read_b32 %1, a51\n\tv_mul_f32 %1, %2, %1\n\tv_accvgpr_write_b32 a51, %1\n\t"
    "v_accvgpr_read_b32 %0, a52\n\tv_mul_f32 %0, %2, %0\n\tv_accvgpr_write_b32 a52, %0\n\t"
    "v_accvgpr_read_b32 %1, a53\n\tv_mul_f32 %1, %2, %1\n\tv_accvgpr_write_b32 a53, %1\n\t"
    "v_accvgpr_read_b32 %0, a54\n\tv_mul_f32 %0, %2, %0\n\tv_accvgpr_write_b32 a54, %0\n\t"
    "v_accvgpr_read_b32 %1, a55\n\tv_mul_f32 %1, %2, %1\n\tv_accvgpr_write_b32 a55, %1\n\t"
    "v_accvgpr_read_b32 %0, a56\n\tv_mul_f32 %0, %2, %0\n\tv_accvgpr_write_b32 a56, %0\n\t"
    "v_accvgpr_read_b32 %1, a57\n\tv_mul_f32 %1, %2, %1\n\tv_accvgpr_write_b32 a57, %1\n\t"
    "v_accvgpr_read_b32 %0, a58\n\tv_mul_f32 %0, %2, %0\n\tv_accvgpr_write_b32 a58, %0\n\t"
    "v_accvgpr_read_b32 %1, a59\n\tv_mul_f32 %1, %2, %1\n\tv_accvgpr_write_b32 a59, %1\n\t"
    "v_accvgpr_read_b32 %0, a60\n\tv_mul_f32 %0, %2, %0\n\tv_accvgpr_write_b32 a60, %0\n\t"
    "v_accvgpr_read_b32 %1, a61\n\tv_mul_f32 %1, %2, %1\n\tv_accvgpr_write_b32 a61, %1\n\t"
    "v_accvgpr_read_b32 %0, a62\n\tv_mul_f32 %0, %2, %0\n\tv_accvgpr_write_b32 a62, %0\n\t"
    "v_accvgpr_read_b32 %1, a63\n\tv_mul_f32 %1, %2, %1\n\tv_accvgpr_write_b32 a63, %1\n\t"
    "v_accvgpr_read_b32 %0, a64\n\tv_mul_f32 %0, %2, %0\n\tv_accvgpr_write_b32 a64, %0\n\t"
    "v_accvgpr_read_b32 %1, a65\n\tv_mul_f32 %1, %2, %1\n\tv_accvgpr_write_b32 a65, %1\n\t"
    "v_accvgpr_read_b32 %0, a66\n\tv_mul_f32 %0, %2, %0\n\tv_accvgpr_write_b32 a66, %0\n\t"
    "v_accvgpr_read_b32 %1, a67\n\tv_mul_f32 %1, %2, %1\n\tv_accvgpr_write_b32 a67, %1\n\t"
    "v_accvgpr_read_b32 %0, a68\n\tv_mul_f32 %0, %2, %0\n\tv_accvgpr_write_b32 a68, %0\n\t"
    "v_accvgpr_read_b32 %1, a69\n\tv_mul_f32 %1, %2, %1\n\tv_accvgpr_write_b32 a69, %1\n\t"
    "v_accvgpr_read_b32 %0, a70\n\tv_mul_f32 %0, %2, %0\n\tv_accvgpr_write_b32 a70, %0\n\t"
    "v_accvgpr_read_b32 %1, a71\n\tv_mul_f32 %1, %2, %1\n\tv_accvgpr_write_b32 a71, %1\n\t"
    "v_accvgpr_read_b32 %0, a72\n\tv_mul_f32 %0, %2, %0\n\tv_accvgpr_write_b32 a72, %0\n\t"
    "v_accvgpr_read_b32 %1, a73\n\tv_mul_f32 %1, %2, %1\n\tv_accvgpr_write_b32 a73, %1\n\t"
    "v_accvgpr_read_b32 %0, a74\n\tv_mul_f32 %0, %2, %0\n\tv_accvgpr_write_b32 a74, %0\n\t"
    "v_accvgpr_read_b32 %1, a75\n\tv_mul_f32 %1, %2, %1\n\tv_accvgpr_write_b32 a75, %1\n\t"
    "v_accvgpr_read_b32 %0, a76\n\tv_mul_f32 %0, %2, %0\n\tv_accvgpr_write_b32 a76, %0\n\t"
    "v_accvgpr_read_b32 %1, a77\n\tv_mul_f32 %1, %2, %1\n\tv_accvgpr_write_b32 a77, %1\n\t"
    "v_accvgpr_read_b32 %0, a78\n\tv_mul_f32 %0, %2, %0\n\tv_accvgpr_write_b32 a78, %0\n\t"
    "v_accvgpr_read_b32 %1, a79\n\tv_mul_f32 %1, %2, %1\n\tv_accvgpr_write_b32 a79, %1\n\t"
    "v_accvgpr_read_b32 %0, a80\n\tv_mul_f32 %0, %2, %0\n\tv_accvgpr_write_b32 a80, %0\n\t"
    "v_accvgpr_read_b32 %1, a81\n\tv_mul_f32 %1, %2, %1\n\tv_accvgpr_write_b32 a81, %1\n\t"
    "v_accvgpr_read_b32 %0, a82\n\tv_mul_f32 %0, %2, %0\n\tv_accvgpr_write_b32 a82, %0\n\t"
    "v_accvgpr_read_b32 %1, a83\n\tv_mul_f32 %1, %2, %1\n\tv_accvgpr_write_b32 a83, %1\n\t"
    "v_accvgpr_read_b32 %0, a84\n\tv_mul_f32 %0, %2, %0\n\tv_accvgpr_write_b32 a84, %0\n\t"
    "v_accvgpr_read_b32 %1, a85\n\tv_mul_f32 %1, %2, %1\n\tv_accvgpr_write_b32 a85, %1\n\t"
    "v_accvgpr_read_b32 %0, a86\n\tv_mul_f32 %0, %2, %0\n\tv_accvgpr_write_b32 a86, %0\n\t"
    "v_accvgpr_read_b32 %1, a87\n\tv_mul_f32 %1, %2, %1\n\tv_accvgpr_write_b32 a87, %1\n\t"
    "v_accvgpr_read_b32 %0, a88\n\tv_mul_f32 %0, %2, %0\n\tv_accvgpr_write_b32 a88, %0\n\t"
    "v_accvgpr_read_b32 %1, a89\n\tv_mul_f32 %1, %2, %1\n\tv_accvgpr_write_b32 a89, %1\n\t"
    "v_accvgpr_read_b32 %0, a90\n\tv_mul_f32 %0, %2, %0\n\tv_accvgpr_write_b32 a90, %0\n\t"
    "v_accvgpr_read_b32 %1, a91\n\tv_mul_f32 %1, %2, %1\n\tv_accvgpr_write_b32 a91, %1\n\t"
    "v_accvgpr_read_b32 %0, a92\n\tv_mul_f32 %0, %2, %0\n\tv_accvgpr_write_b32 a92, %0\n\t"
    "v_accvgpr_read_b32 %1, a93\n\tv_mul_f32 %1, %2, %1\n\tv_accvgpr_write_b32 a93, %1\n\t"
    "v_accvgpr_read_b32 %0, a94\n\tv_mul_f32 %0, %2, %0\n\tv_accvgpr_write_b32 a94, %0\n\t"
    "v_accvgpr_read_b32 %1, a95\n\tv_mul_f32 %1, %2, %1\n\tv_accvgpr_write_b32 a95, %1\n\t"
    "v_accvgpr_read_b32 %0, a96\n\tv_mul_f32 %0, %2, %0\n\tv_accvgpr_write_b32 a96, %0\n\t"
    "v_accvgpr_read_b32 %1, a97\n\tv_mul_f32 %1, %2, %1\n\tv_accvgpr_write_b32 a97, %1\n\t"
    "v_accvgpr_read_b32 %0, a98\n\tv_mul_f32 %0, %2, %0\n\tv_accvgpr_write_b32 a98, %0\n\t"
    "v_accvgpr_read_b32 %1, a99\n\tv_mul_f32 %1, %2, %1\n\tv_accvgpr_write_b32 a99, %1\n\t"
    "v_accvgpr_read_b32 %0, a100\n\tv_mul_f32 %0, %2, %0\n\tv_accvgpr_write_b32 a100, %0\n\t"
    "v_accvgpr_read_b32 %1, a101\n\tv_mul_f32 %1, %2, %1\n\tv_accvgpr_write_b32 a101, %1\n\t"
    "v_accvgpr_read_b32 %0, a102\n\tv_mul_f32 %0, %2, %0\n\tv_accvgpr_write_b32 a102, %0\n\t"
    "v_accvgpr_read_b32 %1, a103\n\tv_mul_f32 %1, %2, %1\n\tv_accvgpr_write_b32 a103, %1\n\t"
    "v_accvgpr_read_b32 %0, a104\n\tv_mul_f32 %0, %2, %0\n\tv_accvgpr_write_b32 a104, %0\n\t"
    "v_accvgpr_read_b32 %1, a105\n\tv_mul_f32 %1, %2, %1\n\tv_accvgpr_write_b32 a105, %1\n\t"
    "v_accvgpr_read_b32 %0, a106\n\tv_mul_f32 %0, %2, %0\n\tv_accvgpr_write_b32 a106, %0\n\t"
    "v_accvgpr_read_b32 %1, a107\n\tv_mul_f32 %1, %2, %1\n\tv_accvgpr_write_b32 a107, %1\n\t"
    "v_accvgpr_read_b32 %0, a108\n\tv_mul_f32 %0, %2, %0\n\tv_accvgpr_write_b32 a108, %0\n\t"
    "v_accvgpr_read_b32 %1, a109\n\tv_mul_f32 %1, %2, %1\n\tv_accvgpr_write_b32 a109, %1\n\t"
    "v_accvgpr_read_b32 %0, a110\n\tv_mul_f32 %0, %2, %0\n\tv_accvgpr_write_b32 a110, %0\n\t"
    "v_accvgpr_read_b32 %1, a111\n\tv_mul_f32 %1, %2, %1\n\tv_accvgpr_write_b32 a111, %1\n\t"
    "v_accvgpr_read_b32 %0, a112\n\tv_mul_f32 %0, %2, %0\n\tv_accvgpr_write_b32 a112, %0\n\t"
    "v_accvgpr_read_b32 %1, a113\n\tv_mul_f32 %1, %2, %1\n\tv_accvgpr_write_b32 a113, %1\n\t"
    "v_accvgpr_read_b32 %0, a114\n\tv_mul_f32 %0, %2, %0\n\tv_accvgpr_write_b32 a114, %0\n\t"
    "v_accvgpr_read_b32 %1, a115\n\tv_mul_f32 %1, %2, %1\n\tv_accvgpr_write_b32 a115, %1\n\t"
    "v_accvgpr_read_b32 %0, a116\n\tv_mul_f32 %0, %2, %0\n\tv_accvgpr_write_b32 a116, %0\n\t"
    "v_accvgpr_read_b32 %1, a117\n\tv_mul_f32 %1, %2, %1\n\tv_accvgpr_write_b32 a117, %1\n\t"
    "v_accvgpr_read_b32 %0, a118\n\tv_mul_f32 %0, %2, %0\n\tv_accvgpr_write_b32 a118, %0\n\t"
    "v_accvgpr_read_b32 %1, a119\n\tv_mul_f32 %1, %2, %1\n\tv_accvgpr_write_b32 a119, %1\n\t"
    "v_accvgpr_read_b32 %0, a120\n\tv_mul_f32 %0, %2, %0\n\tv_accvgpr_write_b32 a120, %0\n\t"
    "v_accvgpr_read_b32 %1, a121\n\tv_mul_f32 %1, %2, %1\n\tv_accvgpr_write_b32 a121, %1\n\t"
    "v_accvgpr_read_b32 %0, a122\n\tv_mul_f32 %0, %2, %0\n\tv_accvgpr_write_b32 a122, %0\n\t"
    "v_accvgpr_read_b32 %1, a123\n\tv_mul_f32 %1, %2, %1\n\tv_accvgpr_write_b32 a123, %1\n\t"
    "v_accvgpr_read_b32 %0, a124\n\tv_mul_f32 %0, %2, %0\n\tv_accvgpr_write_b32 a124, %0\n\t"
    "v_accvgpr_read_b32 %1, a125\n\tv_mul_f32 %1, %2, %1\n\tv_accvgpr_write_b32 a125, %1\n\t"
    "v_accvgpr_read_b32 %0, a126\n\tv_mul_f32 %0, %2, %0\n\tv_accvgpr_write_b32 a126, %0\n\t"
    "v_accvgpr_read_b32 %1, a127\n\tv_mul_f32 %1, %2, %1\n\tv_accvgpr_write_b32 a127, %1\n\t"
    "s_nop 1" : "=&v"(t0), "=&v"(t1) : "v"(alpha) : ACLOB);
  }
}
template <int XC, bool DOQK, bool MASK, int... Ms> __device__ __forceinline__ void attn_step_(AW& w, iseq<Ms...>) {
  constexpr int XN = XC ^ 1;
  w.rs0 = 0.f; w.rs1 = 0.f; w.pm0 = w.ninf; w.pm1 = w.ninf;
  asm volatile("" : "+v"(w.rs0), "+v"(w.rs1), "+v"(w.pm0), "+v"(w.pm1));
  if constexpr (DOQK) k_pre<XN>(w, make_iseq<PD>{});
  GAP();
  (gapA<XC, DOQK, Ms>(w), ...);
  { unsigned pk;
    asm volatile("s_nop 0\n\tv_add_f32 %0, %0, %3\n\tv_cvt_pk_bf16_f32 %1, %2, %3" : "+v"(w.rs1), "=&v"(pk) : "v"(w.pe[30]), "v"(w.pe[31]));
    w.P[3][3] = pk; }
  { float ps = w.rs0 + w.rs1;
    auto rr = __builtin_amdgcn_permlane32_swap(__float_as_uint(ps), __float_as_uint(ps), false, false);
    w.l += __uint_as_float(rr[0]) + __uint_as_float(rr[1]); }
  asm volatile("s_nop 1" ::: "memory");
  GAP();
  (gapB<XC, DOQK, MASK, Ms>(w), ...);
  if constexpr (DOQK) {
    float a = fmaxf(w.pm0, w.pm1);
    auto rr = __builtin_amdgcn_permlane32_swap(__float_as_uint(a), __float_as_uint(a), false, false);
    w.pmax = fmaxf(__uint_as_float(rr[0]), __uint_as_float(rr[1]));
    attn_decide<XN>(w);
    w.sk += 32768; w.sv += 32768; w.thr -= 64;
    asm volatile("s_waitcnt vmcnt(0)" ::: "memory");
    __builtin_amdgcn_s_barrier();
    GAP();
  }
}
template <int XC, bool DOQK, bool MASK> __device__ __forceinline__ void attn_step(AW& w) { attn_step_<XC, DOQK, MASK>(w, make_iseq<32>{}); }

template <int M> __device__ __forceinline__ void qk_first1(AW& w) {
  qk_mfma<0, M, wcnt<M>(), true>(w);
  if constexpr (M + PD < 32) k_load<0, M + PD>(w);
}
template <int... Ms> __device__ __forceinline__ void qk_first(AW& w, iseq<Ms...>) {
  k_pre<0>(w, make_iseq<PD>{});
  (qk_first1<Ms>(w), ...);
}
template <int I> __device__ __forceinline__ void q_load_agpr1(const u16* qp) {
  asm volatile("global_load_dwordx4 a[%c1:%c2], %0, off offset:%c3" :: "v"(qp + (I >> 2) * 2048), "i"(128 + 4 * I), "i"(131 + 4 * I), "i"((I & 3) * 1024) : "memory", ACLOB);
}
template <int... Is> __device__ __forceinline__ void q_load_agpr(const u16* qp, iseq<Is...>) { (q_load_agpr1<Is>(qp), ...); }
__device__ __forceinline__ void pro_dma4(AW& w, unsigned m0v, unsigned soff) {
  asm volatile("s_mov_b32 m0, %0\n\ts_nop 0\n\tbuffer_load_dwordx4 %1, %2, %3 offen lds\n\t"
               "buffer_load_dwordx4 %1, %2, %3 offen offset:1024 lds\n\tbuffer_load_dwordx4 %1, %2, %3 offen offset:2048 lds\n\t"
               "buffer_load_dwordx4 %1, %2, %3 offen offset:3072 lds"
               :: "s"(m0v), "v"(w.voff), "s"(w.srd), "s"(soff) : "memory");
}
template <int I> __device__ __forceinline__ void o_zero1() { asm volatile("v_accvgpr_write_b32 a[%c0], 0" :: "i"(I) : ACLOB); }
template <int... Is> __device__ __forceinline__ void o_zero(iseq<Is...>) { (o_zero1<Is>(), ...); }
template <int I> __device__ __forceinline__ f32x4 o_read4() {
  f32x4 r;
  asm volatile("v_accvgpr_read_b32 %0, a[%c4]\n\tv_accvgpr_read_b32 %1, a[%c5]\n\tv_accvgpr_read_b32 %2, a[%c6]\n\tv_accvgpr_read_b32 %3, a[%c7]"
               : "=v"(r[0]), "=v"(r[1]), "=v"(r[2]), "=v"(r[3]) : "i"(4 * I), "i"(4 * I + 1), "i"(4 * I + 2), "i"(4 * I + 3) : ACLOB);
  return r;
}

__device__ __forceinline__ void attn_segment(AW& w, const u16* __restrict__ Qb, const u16* __restrict__ Kh,
                                             const u16* __restrict__ Vh, int h, int qb, int t0, int t1) {
  const int tid = threadIdx.x;
  const int q = qb * 128 + w.wid * 32 + (tid & 31);
  w.q = q;
  __syncthreads();
  const int n = t1 - t0;
  {
    q_load_agpr(Qb + (size_t)((q >> 5) * 16 + h) * 8192 + (tid & 63) * 8, make_iseq<16>{});
    const unsigned sk0 = (unsigned)(h * S_ * 512 + t0 * 32768);
    const unsigned sv0 = (unsigned)(WS_VT - WS_K) + sk0;
    const unsigned sk1 = sk0 + (n > 1 ? 32768u : 0u);
#pragma unroll
    for (int g = 0; g < 2; ++g) pro_dma4(w, w.m0b + g * 4096u, sk0 + g * 4096u);
#pragma unroll
    for (int g = 0; g < 2; ++g) pro_dma4(w, w.m0b + 65536u + g * 4096u, sv0 + g * 4096u);
#pragma unroll
    for (int g = 0; g < 2; ++g) pro_dma4(w, w.m0b + 32768u + g * 4096u, sk1 + g * 4096u);
    w.sk = sk0 + 65536u;
    w.sv = sv0 + 32768u;
  }
  o_zero(make_iseq<128>{});
  w.m = -1e30f; w.l = 0.f;
  asm volatile("s_waitcnt vmcnt(16)" ::: "memory");
  __builtin_amdgcn_s_barrier();
  GAP();
  qk_first(w, make_iseq<32>{});
  asm volatile("s_nop 15" ::: "memory");
  GAP();
  {
    const int k0 = t0 * 64;
    float a = -INFINITY;
#pragma unroll
    for (int r = 0; r < 16; ++r) {
      float x0 = w.S[0][0][r], x1 = w.S[0][1][r];
      if (k0 + crow(r, w.hi) > q) x0 = -INFINITY;
      if (k0 + 32 + crow(r, w.hi) > q) x1 = -INFINITY;
      w.S[0][0][r] = x0; w.S[0][1][r] = x1;
      a = fmaxf(a, fmaxf(x0, x1));
    }
    auto rr = __builtin_amdgcn_permlane32_swap(__float_as_uint(a), __float_as_uint(a), false, false);
    w.pmax = fmaxf(__uint_as_float(rr[0]), __uint_as_float(rr[1]));
    w.m = fmaxf(w.m, w.pmax);
#pragma unroll
    for (int r = 0; r < 16; ++r) { w.S[0][0][r] -= w.m; w.S[0][1][r] -= w.m; w.negm[r] = -w.m; }
  }
  w.thr = q - (t0 + 1) * 64 - 4 * w.hi;
  asm volatile("s_waitcnt vmcnt(0)" ::: "memory");
  __builtin_amdgcn_s_barrier();
  GAP();
  const int jm = 2 * qb - t0;
  int jj = 0;
  for (; jj + 1 < n; jj += 2) {
    if (jj + 2 >= n) w.sk -= 32768;
    if (jj + 1 < jm) attn_step<0, true, false>(w); else attn_step<0, true, true>(w);
    if (jj + 2 < n) {
      if (jj + 3 >= n) w.sk -= 32768;
      if (jj + 2 < jm) attn_step<1, true, false>(w); else attn_step<1, true, true>(w);
    } else { attn_step<1, false, false>(w); jj = n; break; }
  }
  if (jj < n) attn_step<0, false, false>(w);
  asm volatile("s_nop 15" ::: "memory");
}

__device__ __forceinline__ void ctx_put(u16* dst, int p, const f32x4& v, const f32x4& u) {
  unsigned a0 = pk2(v[0], v[1]), a1 = pk2(v[2], v[3]), b0 = pk2(u[0], u[1]), b1 = pk2(u[2], u[3]);
  auto r0 = __builtin_amdgcn_permlane32_swap(a0, b0, false, false);
  auto r1 = __builtin_amdgcn_permlane32_swap(a1, b1, false, false);
  u32x4 t = {r0[0], r1[0], r0[1], r1[1]};
  *reinterpret_cast<u32x4*>(dst + p * 512) = t;
}
template <int P> __device__ __forceinline__ void ctx_store1(u16* dst, float inv) {
  f32x4 v = o_read4<2 * P>(), u = o_read4<2 * P + 1>();
  v *= inv; u *= inv;
  ctx_put(dst, P, v, u);
}
template <int... Ps> __device__ __forceinline__ void ctx_store(u16* dst, float inv, iseq<Ps...>) { (ctx_store1<Ps>(dst, inv), ...); }
template <int I> __device__ __forceinline__ void part_store1(__amdgpu_buffer_rsrc_t rs, unsigned off) {
  const f32x4 v = o_read4<I>();
  __builtin_amdgcn_raw_buffer_store_b128(__builtin_bit_cast(u32x4, v), rs, off + I * 1024, 0, 16);
}
template <int... Is> __device__ __forceinline__ void part_store(__amdgpu_buffer_rsrc_t rs, unsigned off, iseq<Is...>) { (part_store1<Is>(rs, off), ...); }
template <int P> __device__ __forceinline__ void comb_store1(u16* dst, const f32x4* pu, float a1, float a2) {
  f32x4 v = o_read4<2 * P>(), u = o_read4<2 * P + 1>();
  v = v * a1 + pu[2 * P] * a2; u = u * a1 + pu[2 * P + 1] * a2;
  ctx_put(dst, P, v, u);
}
template <int... Ps> __device__ __forceinline__ void comb_store(u16* dst, __amdgpu_buffer_rsrc_t rs, unsigned off, float a1, float a2, iseq<Ps...>) {
  f32x4 pu[32];
#pragma unroll
  for (int i = 0; i < 32; ++i) pu[i] = __builtin_bit_cast(f32x4, __builtin_amdgcn_raw_buffer_load_b128(rs, off + i * 1024, 0, 16));
  (comb_store1<Ps>(dst, pu, a1, a2), ...);
}

__global__ __launch_bounds__(256, 1) void attn_kernel(char* __restrict__ ws) {
  extern __shared__ __attribute__((aligned(16))) char smem[];
  const u16* Qb = (const u16*)(ws + WS_Q);
  const u16* Kb = (const u16*)(ws + WS_K);
  const u16* Vt = (const u16*)(ws + WS_VT);
  u16* ctx = (u16*)(ws + WS_CTX);
  unsigned* cnt = (unsigned*)(ws + WS_CNT);
  const int xg = blockIdx.x & 7, r = blockIdx.x >> 3;
  const int h = xg * 2 + (r & 1), rr = r >> 1;
  const int i = rr >> 1, side = rr & 1;
  const u16* Kh = Kb + (size_t)h * S_ * 256;
  const u16* Vh = Vt + (size_t)h * S_ * 256;
  const int tid = threadIdx.x, wid = __builtin_amdgcn_readfirstlane(tid >> 6), lane = tid & 63, r32 = lane & 31, hi = lane >> 5;
  AW w;
  w.smem = smem; w.wid = wid; w.hi = hi; w.ninf = -INFINITY;
  const unsigned sb = (unsigned)(size_t)smem;
  w.voff = (unsigned)(wid * 8192 + lane * 16);
  w.m0b = sb + (unsigned)wid * 8192u;
  w.srd = __builtin_amdgcn_make_buffer_rsrc(ws + WS_K, 0, (int)(2 * S_ * HD_ * 2), 0x00020000);
#pragma unroll
  for (int s = 0; s < 8; ++s) w.ka[s] = sb + r32 * 512 + (((s * 2 + hi) ^ (r32 & 15)) << 4);
#pragma unroll
  for (int s = 0; s < 4; ++s) w.va[s] = sb + 65536 + r32 * 128 + (((s * 2 + hi) ^ ((r32 >> 1) & 7)) << 4);
  const int qb = 15 - i;
  const int tsplit = min(TSPLIT, 2 * (16 - i));
  const bool nosplit = tsplit == 2 * (16 - i);
  const int pair = h * 8 + i;
  const __amdgpu_buffer_rsrc_t prs = __builtin_amdgcn_make_buffer_rsrc(ws + WS_PART, 0, (int)(WS_END - WS_PART), 0x00020000);
  const unsigned poff = (unsigned)pair * 131072u + (unsigned)wid * 32768u + (unsigned)lane * 16u;
  const unsigned mloff = (unsigned)(WS_PML - WS_PART) + (unsigned)(pair * 256 + tid) * 8u;
  for (int seg = (side && !nosplit) ? 0 : 1; seg < 2; ++seg) {
    const bool part = side && seg == 0;
    const int sqb = (side && seg == 1) ? i : qb;
    const int t0 = part ? tsplit : 0;
    const int t1 = part ? 2 * (16 - i) : (side ? 2 * (i + 1) : tsplit);
    attn_segment(w, Qb, Kh, Vh, h, sqb, t0, t1);
    if (part) {
      part_store(prs, poff, make_iseq<32>{});
      { u32x2 t = {__float_as_uint(w.m), __float_as_uint(w.l)}; __builtin_amdgcn_raw_buffer_store_b64(t, prs, mloff, 0, 16); }
      asm volatile("s_waitcnt vmcnt(0)" ::: "memory");
      __syncthreads();
      if (tid == 0) __hip_atomic_store(&cnt[128 + pair], 1u, __ATOMIC_RELAXED, __HIP_MEMORY_SCOPE_AGENT);
    } else if (side || nosplit) {
      ctx_store(ctx + (size_t)((w.q >> 5) * 16 + h) * 8192 + lane * 8, 1.f / w.l, make_iseq<16>{});
    }
  }
  if (side == 0 && !nosplit) {
    if (tid == 0) {
      while (__hip_atomic_load(&cnt[128 + pair], __ATOMIC_RELAXED, __HIP_MEMORY_SCOPE_AGENT) == 0u) __builtin_amdgcn_s_sleep(2);
    }
    __syncthreads();
    __builtin_amdgcn_fence(__ATOMIC_ACQUIRE, "wavefront");
    const u32x2 mlw = __builtin_amdgcn_raw_buffer_load_b64(prs, mloff, 0, 16);
    const float om = __uint_as_float(mlw[0]), ol = __uint_as_float(mlw[1]);
    const float mn = fmaxf(w.m, om);
    float a1 = __builtin_amdgcn_exp2f(w.m - mn), a2 = __builtin_amdgcn_exp2f(om - mn);
    const float inv = 1.f / (w.l * a1 + ol * a2);
    a1 *= inv; a2 *= inv;
    comb_store(ctx + (size_t)((w.q >> 5) * 16 + h) * 8192 + lane * 8, prs, poff, a1, a2, make_iseq<16>{});
  }
}

__device__ __forceinline__ unsigned offB64(unsigned row, unsigned ch) {
  const unsigned f = ((row >> 1) & 1) | (((row >> 3) & 1) << 1);
  return 128u * row + 16u * (ch ^ (f << 1));
}
#define OSPLIT 42
constexpr int ONS = 3;
struct OutCtx {
  const u16* ctx; const u16* act; const u16* wo; const u16* w2; char* smem; int m0, n0;
  const u16* pa[2]; const u16* pb; int astep; unsigned lb;
};
__device__ __forceinline__ void out_ptrs(OutCtx& c, int g) {
  const int tid = threadIdx.x;
#pragma unroll
  for (int i = 0; i < 2; ++i) {
    const int p = i * 512 + tid;
    const int row = p >> 3, chs = (p & 7) ^ ((row >> 1) & 7);
    const int gr = c.m0 + row;
    if (g < 64) c.pa[i] = c.ctx + (size_t)((c.m0 >> 5) + (p >> 8)) * 131072 + (size_t)g * 2048 + ((p >> 6) & 3) * 512 + (p & 63) * 8;
    else c.pa[i] = c.act + (size_t)gr * F_ + (g - 64) * 64 + chs * 8;
  }
  c.astep = g < 64 ? 2048 : 64;
  {
    const int p = tid;
    const int row = p >> 3; const unsigned f = ((row >> 1) & 1) | (((row >> 3) & 1) << 1);
    const int chs = (p & 7) ^ (f << 1);
    c.pb = (g < 64 ? c.wo + (size_t)(g * 64 + row) * M_ : c.w2 + (size_t)((g - 64) * 64 + row) * M_) + c.n0 + chs * 8;
  }
}
__device__ __forceinline__ void out_stage(OutCtx& c, int slot, bool adv) {
  const unsigned lb = c.lb + (unsigned)slot * 49152u;
  glds_asm(c.pa[0], lb); glds_asm(c.pa[1], lb + 8192); glds_asm(c.pb, lb + 16384);
  glds_asm(c.pa[0] + c.astep, lb + 24576); glds_asm(c.pa[1] + c.astep, lb + 24576 + 8192); glds_asm(c.pb + 64 * M_, lb + 24576 + 16384);
  if (adv) { c.pa[0] += 2 * c.astep; c.pa[1] += 2 * c.astep; c.pb += 128 * M_; }
}
template <bool TILED>
__device__ __forceinline__ void out_compute(f32x4 (&acc)[2][2], const char* a, int wm, int wn, int lane) {
  const int fr = lane & 15, fq = lane >> 4;
  const char* b = a + 16384;
  bf16x8 af[2][2], bfr[2][2];
#pragma unroll
  for (int ks = 0; ks < 2; ++ks) {
#pragma unroll
    for (int mi = 0; mi < 2; ++mi) {
      const int row = wm * 32 + mi * 16 + fr, ch = ks * 4 + fq;
      if (TILED) af[ks][mi] = *reinterpret_cast<const bf16x8*>(a + ((wm * 4 + (ch >> 1)) * 64 + (ch & 1) * 32 + (row & 31)) * 16);
      else af[ks][mi] = *reinterpret_cast<const bf16x8*>(a + row * 128 + ((ch ^ ((row >> 1) & 7)) << 4));
    }
#pragma unroll
    for (int ni = 0; ni < 2; ++ni) {
      const int c = wn * 2 + ni, q = fr >> 2, pp = lane & 3;
      const unsigned a0 = offB64(32 * ks + 8 * fq + q, 2 * c + (pp >> 1)) + 8 * (pp & 1);
      const unsigned a1 = offB64(32 * ks + 8 * fq + 4 + q, 2 * c + (pp >> 1)) + 8 * (pp & 1);
      s16x4 lo = __builtin_amdgcn_ds_read_tr16_b64_v4i16((lds_s16x4*)(b + a0));
      s16x4 hi = __builtin_amdgcn_ds_read_tr16_b64_v4i16((lds_s16x4*)(b + a1));
      bfr[ks][ni] = bf16x8{lo[0], lo[1], lo[2], lo[3], hi[0], hi[1], hi[2], hi[3]};
    }
  }
#pragma unroll
  for (int ks = 0; ks < 2; ++ks)
#pragma unroll
    for (int mi = 0; mi < 2; ++mi)
#pragma unroll
      for (int ni = 0; ni < 2; ++ni)
        acc[mi][ni] = __builtin_amdgcn_mfma_f32_16x16x32_bf16(bfr[ks][ni], af[ks][mi], acc[mi][ni], 0, 0, 0);
}
template <bool TILED>
__device__ __forceinline__ void out_run(f32x4 (&acc)[2][2], OutCtx& c, int g0, int g1, int gend, int& slot, int wm, int wn, int lane) {
  for (int g = g0; g < g1; g += 2) {
    asm volatile("s_waitcnt vmcnt(%c0)" :: "i"(6 * (ONS - 2)) : "memory");
    __builtin_amdgcn_s_barrier();
    __builtin_amdgcn_sched_barrier(0);
    const int gl = g + 2 * (ONS - 1);
    if (gl == 64) out_ptrs(c, 64);
    int ls = slot + ONS - 1; if (ls >= ONS) ls -= ONS;
    out_stage(c, ls, gl + 2 < gend);
    out_compute<TILED>(acc, c.smem + slot * 49152, wm, wn, lane);
    out_compute<TILED>(acc, c.smem + slot * 49152 + 24576, wm, wn, lane);
    asm volatile("s_waitcnt lgkmcnt(0)" ::: "memory");
    __builtin_amdgcn_sched_barrier(0);
    if (++slot == ONS) slot = 0;
  }
}

__global__ __launch_bounds__(512, 1) void out_kernel(const u16* __restrict__ x, const u16* __restrict__ wo, const u16* __restrict__ w2,
                                                     char* __restrict__ ws, u16* __restrict__ out) {
  extern __shared__ __attribute__((aligned(16))) char smem[];
  const int bx = blockIdx.x, xg = bx & 7, jx = bx >> 3;
  const int half = xg & 1, rb = (xg >> 1) * 4 + (jx & 3), cb = jx >> 2;
  const int tile = cb * 16 + rb;
  const int m0 = rb * 128, n0 = cb * 64;
  const int tid = threadIdx.x, wid = tid >> 6, lane = tid & 63;
  const int wm = wid >> 1, wn = wid & 1, fr = lane & 15, fq = lane >> 4;
  OutCtx c;
  c.ctx = (const u16*)(ws + WS_CTX); c.act = (const u16*)(ws + WS_ACT); c.wo = wo; c.w2 = w2; c.smem = smem; c.m0 = m0; c.n0 = n0;
  c.lb = __builtin_amdgcn_readfirstlane((unsigned)(size_t)smem + (unsigned)wid * 1024u);
  f32x4 accs[2][2], accg[2][2];
#pragma unroll
  for (int i = 0; i < 2; ++i)
#pragma unroll
    for (int j = 0; j < 2; ++j) { accs[i][j] = f32x4{0.f, 0.f, 0.f, 0.f}; accg[i][j] = f32x4{0.f, 0.f, 0.f, 0.f}; }
  const int gs = half ? OSPLIT : 0;
  out_ptrs(c, gs);
#pragma unroll
  for (int i = 0; i < ONS - 1; ++i) out_stage(c, i, true);
  int slot = 0;
  if (half == 0) {
    out_run<true>(accs, c, 0, OSPLIT, OSPLIT, slot, wm, wn, lane);
  } else {
    out_run<true>(accs, c, OSPLIT, 64, 92, slot, wm, wn, lane);
    out_run<false>(accg, c, 64, 92, 92, slot, wm, wn, lane);
  }
  asm volatile("s_waitcnt vmcnt(0)" ::: "memory");
  unsigned* cnt = (unsigned*)(ws + WS_CNT);
  const __amdgpu_buffer_rsrc_t prs = __builtin_amdgcn_make_buffer_rsrc(ws + WS_OPART, 0, 112 * 65536, 0x00020000);
  const unsigned poff = (unsigned)tile * 65536u + (unsigned)tid * 16u;
  if (half == 0) {
#pragma unroll
    for (int i = 0; i < 4; ++i) __builtin_amdgcn_raw_buffer_store_b128(__builtin_bit_cast(u32x4, accs[i >> 1][i & 1]), prs, poff + i * 8192, 0, 16);
    asm volatile("s_waitcnt vmcnt(0)" ::: "memory");
    __syncthreads();
    if (tid == 0) __hip_atomic_store(&cnt[384 + tile], 1u, __ATOMIC_RELAXED, __HIP_MEMORY_SCOPE_AGENT);
    return;
  }
  if (tid == 0) {
    while (__hip_atomic_load(&cnt[384 + tile], __ATOMIC_RELAXED, __HIP_MEMORY_SCOPE_AGENT) == 0u) __builtin_amdgcn_s_sleep(2);
  }
  __syncthreads();
  __builtin_amdgcn_fence(__ATOMIC_ACQUIRE, "wavefront");
#pragma unroll
  for (int i = 0; i < 4; ++i) accs[i >> 1][i & 1] += __builtin_bit_cast(f32x4, __builtin_amdgcn_raw_buffer_load_b128(prs, poff + i * 8192, 0, 16));
#pragma unroll
  for (int mi = 0; mi < 2; ++mi)
#pragma unroll
    for (int ni = 0; ni < 2; ++ni) {
      const int m = m0 + wm * 32 + mi * 16 + fr;
      const int n = n0 + wn * 32 + ni * 16 + fq * 4;
      const u32x2 xw = *reinterpret_cast<const u32x2*>(x + (size_t)m * M_ + n);
      float r[4];
#pragma unroll
      for (int j = 0; j < 4; ++j) {
        const float xv = (j & 1) ? __uint_as_float(xw[j >> 1] & 0xffff0000u) : __uint_as_float(xw[j >> 1] << 16);
        const float sb = rbf(accs[mi][ni][j]), gb = rbf(accg[mi][ni][j]);
        const float sg = rbf(gb / (1.f + __expf(-gb)));
        r[j] = xv + rbf(sb * sg);
      }
      u32x2 w = {pk2(r[0], r[1]), pk2(r[2], r[3])};
      *reinterpret_cast<u32x2*>(out + (size_t)m * M_ + n) = w;
    }
}

extern "C" void kernel_launch(void* const* d_in, const int* in_sizes, int n_in,
                              void* d_out, int out_size, void* d_ws, size_t ws_size,
                              hipStream_t stream) {
  const u16* x   = (const u16*)d_in[0];
  const u16* ckv = (const u16*)d_in[1];
  const u16* wq  = (const u16*)d_in[2];
  const u16* wuk = (const u16*)d_in[3];
  const u16* wuv = (const u16*)d_in[4];
  const u16* wo  = (const u16*)d_in[5];
  const u16* w1  = (const u16*)d_in[7];
  const u16* b1  = (const u16*)d_in[8];
  const u16* w2  = (const u16*)d_in[9];
  char* ws = (char*)d_ws;
  (void)hipFuncSetAttribute((const void*)attn_kernel, hipFuncAttributeMaxDynamicSharedMemorySize, 131072);
  (void)hipFuncSetAttribute((const void*)proj_kernel, hipFuncAttributeMaxDynamicSharedMemorySize, 131072);
  (void)hipFuncSetAttribute((const void*)out_kernel, hipFuncAttributeMaxDynamicSharedMemorySize, ONS * 49152);
  ln_kernel<<<S_ / 4, 256, 0, stream>>>(x, (u16*)(ws + WS_XN), (unsigned*)(ws + WS_CNT));
  proj_kernel<<<440, 512, 131072, stream>>>((const u16*)(ws + WS_XN), ckv, wq, wuk, wuv, w1, b1, ws);
  attn_kernel<<<256, 256, 131072, stream>>>(ws);
  out_kernel<<<224, 512, ONS * 49152, stream>>>(x, wo, w2, ws, (u16*)d_out);
}
```
